```python
import torch

def _softmax(x, axis=-1):
    return torch.softmax(x, dim=axis)

def _gelu(x):
    return torch.nn.functional.gelu(x, approximate='tanh')

def _silu(x):
    return torch.nn.functional.silu(x)

def _sigmoid(x):
    return torch.sigmoid(x)

def _layernorm(x, axis=-1, eps=1e-05):
    m = x.mean(dim=axis, keepdim=True)
    v = x.var(dim=axis, keepdim=True, unbiased=False)
    return (x - m) / torch.sqrt(v + eps)

def _scatter_add(tgt, idx, src, axis=0):
    return tgt.clone().index_add_(axis, idx, src)

def _segment_sum(src, seg, n):
    out = torch.zeros((n,) + src.shape[1:], dtype=src.dtype)
    return out.index_add_(0, seg, src)

def _pad(x, pw):
    flat = tuple((v for pair in reversed(pw) for v in pair))
    return torch.nn.functional.pad(x, flat)
kernel_name = 'interleave_layers__memory_aug_recurrent_mem_tr__linear_attn_block__prompt'

def setup_inputs():

    _w = lambda a: (lambda t: t.to(torch.bfloat16) if t.is_floating_point() else t)(torch.as_tensor(a))

    def _A_setup():
        B, S, Dm = (1, 2048, 512)
        H, Dh = (32, 128)
        M, seg = (8, 32)
        seg = min(seg, S)
        S = S // seg * seg
        return {'x': _w((lambda *s: torch.randn(*s, dtype=torch.bfloat16) * 0.05)(B, S, Dm)), 'mem0': _w((lambda *s: torch.randn(*s, dtype=torch.bfloat16) * 0.2)(B, M, Dm)), 'Wq': _w((lambda *s: torch.randn(*s, dtype=torch.bfloat16) * 1.6)(Dm, H, Dh) * Dm ** (-0.5)), 'Wk': _w((lambda *s: torch.randn(*s, dtype=torch.bfloat16) * 1.6)(Dm, H, Dh) * Dm ** (-0.5)), 'Wv': _w((lambda *s: torch.randn(*s, dtype=torch.bfloat16) * 0.2)(Dm, H, Dh) * Dm ** (-0.5)), 'Wo': _w((lambda *s: torch.randn(*s, dtype=torch.bfloat16) * 0.2)(H, Dh, Dm) * (H * Dh) ** (-0.5)), 'seg_len': seg}
    _A_in = _A_setup()

    def _B_setup():
        B, S, D = (1, 2048, 512)
        return {'x': _w((lambda *s: torch.randn(*s, dtype=torch.bfloat16) * 0.05)(B, S, D)), 'wqkv': _w((lambda *s: torch.randn(*s, dtype=torch.bfloat16) * 0.1)(D, 3 * D)), 'wo': _w((lambda *s: torch.randn(*s, dtype=torch.bfloat16) * 0.05)(D, D))}
    _B_in = _B_setup()
    return {'A_x': _A_in['x'], 'A_mem0': _A_in['mem0'], 'A_Wq': _A_in['Wq'], 'A_Wk': _A_in['Wk'], 'A_Wv': _A_in['Wv'], 'A_Wo': _A_in['Wo'], 'A_seg_len': _A_in['seg_len'], 'B_x': _B_in['x'], 'B_wqkv': _B_in['wqkv'], 'B_wo': _B_in['wo']}

def reference(A_x, A_mem0, A_Wq, A_Wk, A_Wv, A_Wo, A_seg_len, B_x, B_wqkv, B_wo):

    def _A_ref(x, mem0, Wq, Wk, Wv, Wo, seg_len):
        B, S, Dm = x.shape
        H, Dh = (Wq.shape[1], Wq.shape[2])
        M = mem0.shape[1]
        scale = Dh ** (-0.5)
        n_seg = S // seg_len
        L = M + seg_len
        idx = torch.arange(L)
        mask = (idx[:, None] < M) | (idx[:, None] >= idx[None, :])
        mem = mem0
        outs = []
        for s in range(n_seg):
            xs = x[:, s * seg_len:(s + 1) * seg_len, :].clone()
            h = torch.concatenate([mem, xs], axis=1)
            q = torch.einsum('btd,dhe->bhte', h, Wq)
            k = torch.einsum('btd,dhe->bhte', h, Wk)
            v = torch.einsum('btd,dhe->bhte', h, Wv)
            scores = torch.einsum('bhqe,bhke->bhqk', q, k) * scale
            scores = torch.where(mask[None, None], scores, -1000000000.0)
            p = _softmax(scores, axis=-1)
            o = torch.einsum('bhqk,bhke->bhqe', p, v)
            o = torch.einsum('bhte,hed->btd', o, Wo)
            mem = o[:, :M, :].clone()
            outs.append(o[:, M:, :])
        return torch.concatenate(outs, axis=1)

    def _B_ref(x, wqkv, wo):
        B, S, D = x.shape
        H = 32
        Dh = D // H
        qkv = (lambda x, *d: x.permute(*d))((x @ wqkv).reshape(B, S, 3, H, Dh), 2, 0, 3, 1, 4)
        q, k, v = (qkv[0], qkv[1], qkv[2])
        phi = lambda t: torch.where(t > 0, t + 1, torch.exp(t)) + 1e-06
        pq, pk = (phi(q), phi(k))
        kv = torch.einsum('bhsd,bhse->bhde', pk, v)
        z = torch.einsum('bhsd->bhd', pk)
        num = torch.einsum('bhqd,bhde->bhqe', pq, kv)
        den = torch.einsum('bhqd,bhd->bhq', pq, z)[..., None] + 1e-06
        o = num / den
        return (lambda x, *d: x.permute(*d))(o, 0, 2, 1, 3).reshape(B, S, D) @ wo
    x = A_x
    x = x + _A_ref(_layernorm(x, axis=-1), A_mem0, A_Wq, A_Wk, A_Wv, A_Wo, A_seg_len)
    x = x + _B_ref(_layernorm(x, axis=-1), B_wqkv, B_wo)
    x = x + _A_ref(_layernorm(x, axis=-1), A_mem0, A_Wq, A_Wk, A_Wv, A_Wo, A_seg_len)
    x = x + _B_ref(_layernorm(x, axis=-1), B_wqkv, B_wo)
    return x
```

```cpp
#include <hip/hip_runtime.h>
#include <hip/hip_bf16.h>
#include <stdint.h>

typedef unsigned short u16;
typedef unsigned long long u64;
typedef short bf16x8 __attribute__((ext_vector_type(8)));
typedef float f32x4 __attribute__((ext_vector_type(4)));
typedef unsigned v4u __attribute__((ext_vector_type(4)));
typedef unsigned v2u __attribute__((ext_vector_type(2)));

constexpr int S_TOK = 2048, DM = 512, NH = 32, DH = 128, NMEM = 8, SEG = 32, NSEG = 64, HD = NH * DH;

__device__ __forceinline__ float bf2f(u16 b) { return __uint_as_float((unsigned)b << 16); }
__device__ __forceinline__ u16 f2bf(float f) {
  __hip_bfloat16 h = __float2bfloat16(f);
  return __builtin_bit_cast(u16, h);
}
__device__ __forceinline__ float rbf(float f) { return bf2f(f2bf(f)); }
__device__ __forceinline__ unsigned pack2(float a, float b) { return (unsigned)f2bf(a) | ((unsigned)f2bf(b) << 16); }
__device__ __forceinline__ f32x4 mfma16(v4u a, v4u b, f32x4 c) {
  return __builtin_amdgcn_mfma_f32_16x16x32_bf16(__builtin_bit_cast(bf16x8, a), __builtin_bit_cast(bf16x8, b), c, 0, 0, 0);
}

constexpr size_t OFF_ACC = 0;
constexpr size_t SZ_ACC = (size_t)2 * NSEG * 16384;
constexpr size_t OFF_CTL = OFF_ACC + SZ_ACC;
constexpr size_t SZ_CTL = 4096;
constexpr size_t OFF_WT = OFF_CTL + SZ_CTL;
constexpr size_t OFF_WOT = OFF_WT + (size_t)3 * HD * DM * 2;
constexpr size_t OFF_BQKVT = OFF_WOT + (size_t)DM * HD * 2;
constexpr size_t OFF_BOT = OFF_BQKVT + (size_t)3 * DM * DM * 2;
constexpr size_t OFF_XN = OFF_BOT + (size_t)DM * DM * 2;
constexpr size_t OFF_XCUR = OFF_XN + (size_t)S_TOK * DM * 2;
constexpr size_t OFF_Q = OFF_XCUR + (size_t)S_TOK * DM * 2;
constexpr size_t OFF_K = OFF_Q + (size_t)S_TOK * HD * 2;
constexpr size_t OFF_VT = OFF_K + (size_t)S_TOK * HD * 2;
constexpr size_t OFF_MEM = OFF_VT + (size_t)S_TOK * HD * 2;
constexpr size_t OFF_KM = OFF_MEM + (size_t)NSEG * NMEM * DM * 2;
constexpr size_t OFF_VMT = OFF_KM + (size_t)NSEG * NMEM * HD * 2;
constexpr size_t OFF_O = OFF_VMT + (size_t)NSEG * NMEM * HD * 2;
constexpr size_t OFF_PART = OFF_O + (size_t)S_TOK * HD * 2;
constexpr size_t OFF_QKVB = OFF_PART + (size_t)4 * S_TOK * DM * 4;
constexpr size_t OFF_KVP = OFF_QKVB + (size_t)S_TOK * 3 * DM * 2;
constexpr size_t OFF_KV = OFF_KVP + (size_t)64 * 32 * 272 * 4;
constexpr size_t OFF_OB = OFF_KV + (size_t)32 * 272 * 4;
constexpr size_t WS_END = OFF_OB + (size_t)S_TOK * DM * 2;

__global__ __launch_bounds__(256) void k_transpose(const u16* Wq, const u16* Wk, const u16* Wv, const u16* Wo, const u16* Bq, const u16* Bo,
                                                   u16* WT, u16* WoT, u16* BqT, u16* BoT) {
  __shared__ u16 tile[64][66];
  int b = blockIdx.x;
  const u16* src; u16* dst; int R, C;
  if (b < 512) { src = Wq; dst = WT; R = 512; C = 4096; }
  else if (b < 1024) { b -= 512; src = Wk; dst = WT + (size_t)HD * DM; R = 512; C = 4096; }
  else if (b < 1536) { b -= 1024; src = Wv; dst = WT + (size_t)2 * HD * DM; R = 512; C = 4096; }
  else if (b < 2048) { b -= 1536; src = Wo; dst = WoT; R = 4096; C = 512; }
  else if (b < 2240) { b -= 2048; src = Bq; dst = BqT; R = 512; C = 1536; }
  else { b -= 2240; src = Bo; dst = BoT; R = 512; C = 512; }
  int tc = C / 64;
  int r0 = (b / tc) * 64, c0 = (b % tc) * 64;
  int t = threadIdx.x;
  for (int i = 0; i < 2; ++i) {
    int idx = t + i * 256;
    int r = idx / 8, c = (idx % 8) * 8;
    v4u v = *(const v4u*)(src + (size_t)(r0 + r) * C + c0 + c);
    u16* e = (u16*)&v;
    for (int j = 0; j < 8; ++j) tile[r][c + j] = e[j];
  }
  __syncthreads();
  for (int i = 0; i < 2; ++i) {
    int idx = t + i * 256;
    int c = idx / 8, r = (idx % 8) * 8;
    v4u v; u16* e = (u16*)&v;
    for (int j = 0; j < 8; ++j) e[j] = tile[r + j][c];
    *(v4u*)(dst + (size_t)(c0 + c) * R + r0 + r) = v;
  }
}

__global__ __launch_bounds__(512) void k_bounds(const u16* Wv, const u16* Wo, unsigned* ctl) {
  int h = blockIdx.x, t = threadIdx.x;
  __shared__ float red[8];
  float s = 0.f;
  for (int e = 0; e < DH; e += 8) {
    v4u v = *(const v4u*)(Wv + (size_t)t * HD + h * DH + e);
    u16* p = (u16*)&v;
    for (int j = 0; j < 8; ++j) { float x = bf2f(p[j]); s += x * x; }
  }
  for (int o = 32; o; o >>= 1) s += __shfl_xor(s, o);
  if ((t & 63) == 0) red[t >> 6] = s;
  __syncthreads();
  float c = 0.f;
  for (int e = 0; e < DH; ++e) { float x = bf2f(Wo[((size_t)h * DH + e) * DM + t]); c += x * x; }
  for (int o = 32; o; o >>= 1) c = fmaxf(c, __shfl_xor(c, o));
  if ((t & 63) == 0) atomicMax(&ctl[1], __float_as_uint(sqrtf(c) * 1.001f));
  if (t == 0) {
    float tot = 0.f;
    for (int i = 0; i < 8; ++i) tot += red[i];
    atomicMax(&ctl[0], __float_as_uint(sqrtf(tot) * 1.001f));
  }
}

template <int NP, bool DOLN>
__global__ __launch_bounds__(256) void k_ln(const u16* xin, const float* part, u16* xout, u16* xn) {
  int row = blockIdx.x * 4 + (threadIdx.x >> 6), lane = threadIdx.x & 63;
  v4u v = *(const v4u*)(xin + (size_t)row * DM + lane * 8);
  u16* e = (u16*)&v;
  float x[8];
  for (int j = 0; j < 8; ++j) x[j] = bf2f(e[j]);
  if (NP > 0) {
    float a[8] = {0, 0, 0, 0, 0, 0, 0, 0};
    for (int p = 0; p < NP; ++p) {
      const f32x4* pp = (const f32x4*)(part + ((size_t)p * S_TOK + row) * DM + lane * 8);
      f32x4 a0 = pp[0], a1 = pp[1];
      a[0] += a0[0]; a[1] += a0[1]; a[2] += a0[2]; a[3] += a0[3];
      a[4] += a1[0]; a[5] += a1[1]; a[6] += a1[2]; a[7] += a1[3];
    }
    v4u o; u16* oe = (u16*)&o;
    for (int j = 0; j < 8; ++j) { x[j] = rbf(x[j] + rbf(a[j])); oe[j] = f2bf(x[j]); }
    *(v4u*)(xout + (size_t)row * DM + lane * 8) = o;
  }
  if (DOLN) {
    float s = 0.f;
    for (int j = 0; j < 8; ++j) s += x[j];
    for (int o = 32; o; o >>= 1) s += __shfl_xor(s, o);
    float mean = s * (1.f / DM);
    float q = 0.f;
    for (int j = 0; j < 8; ++j) { float d = x[j] - mean; q += d * d; }
    for (int o = 32; o; o >>= 1) q += __shfl_xor(q, o);
    float var = rbf(q * (1.f / DM));
    float mb = rbf(mean);
    float sq = rbf(sqrtf(rbf(var + 1e-5f)));
    v4u o; u16* oe = (u16*)&o;
    for (int j = 0; j < 8; ++j) oe[j] = f2bf(rbf(x[j] - mb) / sq);
    *(v4u*)(xn + (size_t)row * DM + lane * 8) = o;
  }
}

template <int EPI>
__global__ __launch_bounds__(256, 2) void k_gemm(const u16* __restrict__ A, const u16* __restrict__ Bt, int M, int N, int K, int Ksplit,
                                                 u16* C0, u16* C1, u16* C2, float* Cf, int ldc) {
  extern __shared__ __attribute__((aligned(16))) unsigned char smem[];
  const int tid = threadIdx.x, lane = tid & 63, wid = tid >> 6;
  const int wr = wid >> 1, wc = wid & 1, fr = lane & 15, fq = lane >> 4;
  const int brow = blockIdx.y * 128, bcol = blockIdx.x * 128;
  const int k0 = blockIdx.z * Ksplit;
  const int nt = Ksplit / 64;
  auto stage = [&](int t, int buf) {
#pragma unroll
    for (int i = 0; i < 4; ++i) {
      int chunk = i * 256 + tid;
      int r = chunk >> 3, cpos = chunk & 7;
      int csrc = cpos ^ (r & 7);
      const u16* ga = A + (size_t)(brow + r) * K + k0 + t * 64 + csrc * 8;
      const u16* gb = Bt + (size_t)(bcol + r) * K + k0 + t * 64 + csrc * 8;
      unsigned char* la = smem + buf * 32768 + (i * 256 + (tid & ~63)) * 16;
      unsigned char* lb = la + 16384;
      __builtin_amdgcn_global_load_lds((const unsigned*)ga, (unsigned*)la, 16, 0, 0);
      __builtin_amdgcn_global_load_lds((const unsigned*)gb, (unsigned*)lb, 16, 0, 0);
    }
  };
  f32x4 acc[4][4] = {};
  bool swapped = true;
  if (EPI == 2) swapped = bcol < 2 * HD;
  if (EPI == 3) swapped = bcol < HD;
  stage(0, 0);
  for (int t = 0; t < nt; ++t) {
    asm volatile("s_waitcnt vmcnt(0)" ::: "memory");
    __syncthreads();
    if (t + 1 < nt) stage(t + 1, (t + 1) & 1);
    const unsigned char* sa = smem + (t & 1) * 32768;
    const unsigned char* sb = sa + 16384;
#pragma unroll
    for (int ks = 0; ks < 2; ++ks) {
      v4u af[4], bfr[4];
#pragma unroll
      for (int m = 0; m < 4; ++m) { int r = wr * 64 + m * 16 + fr; af[m] = *(const v4u*)(sa + r * 128 + (((ks * 4 + fq) ^ (r & 7)) << 4)); }
#pragma unroll
      for (int n = 0; n < 4; ++n) { int r = wc * 64 + n * 16 + fr; bfr[n] = *(const v4u*)(sb + r * 128 + (((ks * 4 + fq) ^ (r & 7)) << 4)); }
      if (swapped) {
#pragma unroll
        for (int m = 0; m < 4; ++m)
#pragma unroll
          for (int n = 0; n < 4; ++n) acc[m][n] = mfma16(bfr[n], af[m], acc[m][n]);
      } else {
#pragma unroll
        for (int m = 0; m < 4; ++m)
#pragma unroll
          for (int n = 0; n < 4; ++n) acc[m][n] = mfma16(af[m], bfr[n], acc[m][n]);
      }
    }
  }
#pragma unroll
  for (int m = 0; m < 4; ++m)
#pragma unroll
    for (int n = 0; n < 4; ++n) {
      f32x4 a = acc[m][n];
      if (swapped) {
        int row = brow + wr * 64 + m * 16 + fr, col = bcol + wc * 64 + n * 16 + fq * 4;
        if (EPI == 1) {
          *(f32x4*)(Cf + ((size_t)blockIdx.z * M + row) * N + col) = a;
        } else {
          v2u o = {pack2(a[0], a[1]), pack2(a[2], a[3])};
          u16* dst;
          if (EPI == 0) dst = C0 + (size_t)row * ldc + col;
          else if (EPI == 2) dst = (col < HD ? C0 + (size_t)row * HD + col : C1 + (size_t)row * HD + (col - HD));
          else dst = C0 + (size_t)row * HD + col;
          *(v2u*)dst = o;
        }
      } else {
        int row = brow + wr * 64 + m * 16 + fq * 4, col = bcol + wc * 64 + n * 16 + fr;
        v2u o = {pack2(a[0], a[1]), pack2(a[2], a[3])};
        if (EPI == 2) {
          int he = col - 2 * HD;
          int seg = row >> 5, tk = row & 31;
          *(v2u*)(C2 + ((size_t)seg * HD + he) * 32 + tk) = o;
        } else if (EPI == 3) {
          int he = col - HD;
          int seg = row >> 3, tk = row & 7;
          *(v2u*)(C1 + ((size_t)seg * HD + he) * 8 + tk) = o;
        }
      }
    }
}

constexpr int REC_LDS_WV = 0;
constexpr int REC_LDS_MEM = 131072;
constexpr int REC_LDS_SC = REC_LDS_MEM + 8192;
constexpr int REC_LDS_O = REC_LDS_SC + 12288;
constexpr int REC_LDS_NRM = REC_LDS_O + 4096;
constexpr int REC_LDS_BYTES = REC_LDS_NRM + 64;

__global__ __launch_bounds__(256, 1) void k_rec(const u16* __restrict__ WT, const u16* __restrict__ WoT, const u16* __restrict__ mem0,
                                                const u16* __restrict__ Kx, const u16* __restrict__ VT, u64* acc, u16* memall,
                                                unsigned* ctl) {
  extern __shared__ __attribute__((aligned(16))) unsigned char smem[];
  const int tid = threadIdx.x, lane = tid & 63;
  const int w = __builtin_amdgcn_readfirstlane(tid >> 6);
  const int h = blockIdx.x;
  const int fr = lane & 15, g = lane >> 4;
  v4u wq[2][16], wk[2][16], wo[8][4];
#pragma unroll
  for (int t = 0; t < 2; ++t)
#pragma unroll
    for (int kk = 0; kk < 16; ++kk) {
      const u16* p = WT + (size_t)(h * DH + 32 * w + 16 * t + fr) * DM + 32 * kk + 8 * g;
      wq[t][kk] = *(const v4u*)p;
      wk[t][kk] = *(const v4u*)(p + (size_t)HD * DM);
      v4u v = *(const v4u*)(p + (size_t)2 * HD * DM);
      *(v4u*)(smem + REC_LDS_WV + (((w * 2 + t) * 16 + kk) * 64 + lane) * 16) = v;
    }
#pragma unroll
  for (int t = 0; t < 8; ++t)
#pragma unroll
    for (int ks = 0; ks < 4; ++ks) {
      const u16* p = WoT + (size_t)(128 * w + 16 * t + fr) * HD + h * DH + 32 * ks + 4 * g;
      v2u a = *(const v2u*)p, b = *(const v2u*)(p + 16);
      wo[t][ks] = (v4u){a.x, a.y, b.x, b.y};
    }
  const float wbound = __uint_as_float(ctl[0]) * __uint_as_float(ctl[1]);
  const __amdgpu_buffer_rsrc_t racc = __builtin_amdgcn_make_buffer_rsrc(acc, 0, NSEG * 16384, 0x00020000);
  float delta_prev = 0.f;
  const float scale = 0.08838834764831845f;
  v4u kx[2], vx[2];
  auto prefetch = [&](int s) {
#pragma unroll
    for (int t = 0; t < 2; ++t) {
      const u16* p = Kx + (size_t)(s * SEG + 16 * t + fr) * HD + h * DH + 32 * w + 4 * g;
      v2u a = *(const v2u*)p, b = *(const v2u*)(p + 16);
      kx[t] = (v4u){a.x, a.y, b.x, b.y};
      const u16* q = VT + ((size_t)s * HD + h * DH + 32 * w + 16 * t + fr) * 32 + 4 * g;
      v2u c = *(const v2u*)q, d = *(const v2u*)(q + 16);
      vx[t] = (v4u){c.x, c.y, d.x, d.y};
    }
  };
  prefetch(0);
  for (int s = 0; s < NSEG; ++s) {
    v4u mu[2];
    if (s == 0) {
#pragma unroll
      for (int i = 0; i < 2; ++i) { int u = tid + 256 * i; mu[i] = *(const v4u*)(mem0 + (u & 7) * DM + (u >> 3) * 8); }
    } else {
      v4u f[4];
      unsigned spins = 0;
      for (;;) {
        asm volatile("" ::: "memory");
#pragma unroll
        for (int i = 0; i < 2; ++i) {
          int off = s * 16384 + (tid + 256 * i) * 32;
          f[2 * i] = __builtin_amdgcn_raw_buffer_load_b128(racc, off, 0, 16);
          f[2 * i + 1] = __builtin_amdgcn_raw_buffer_load_b128(racc, off + 16, 0, 16);
        }
        bool ok = true;
#pragma unroll
        for (int i = 0; i < 4; ++i) ok &= (f[i].y >> 26) == 32u && (f[i].w >> 26) == 32u;
        if (__all(ok)) break;
        if (++spins > (1u << 20)) { ctl[2] = 1u; break; }
      }
#pragma unroll
      for (int i = 0; i < 2; ++i) {
        float val[8];
#pragma unroll
        for (int j = 0; j < 2; ++j) {
          v4u q = f[2 * i + j];
          int a0 = (int)(q.x & 0x1FFFFFFFu) - (1 << 28);
          int b0 = (int)(((q.x >> 29) | (q.y << 3)) & 0x1FFFFFFFu) - (1 << 28);
          int a1 = (int)(q.z & 0x1FFFFFFFu) - (1 << 28);
          int b1 = (int)(((q.z >> 29) | (q.w << 3)) & 0x1FFFFFFFu) - (1 << 28);
          val[4 * j + 0] = (float)a0 * delta_prev; val[4 * j + 1] = (float)b0 * delta_prev;
          val[4 * j + 2] = (float)a1 * delta_prev; val[4 * j + 3] = (float)b1 * delta_prev;
        }
        mu[i] = (v4u){pack2(val[0], val[1]), pack2(val[2], val[3]), pack2(val[4], val[5]), pack2(val[6], val[7])};
      }
    }
    float ssq = 0.f;
#pragma unroll
    for (int i = 0; i < 2; ++i) {
      int u = tid + 256 * i;
      *(v4u*)(smem + REC_LDS_MEM + u * 16) = mu[i];
      unsigned ww[4] = {mu[i].x, mu[i].y, mu[i].z, mu[i].w};
#pragma unroll
      for (int j = 0; j < 4; ++j) { float a = __uint_as_float(ww[j] << 16), b = __uint_as_float(ww[j] & 0xFFFF0000u); ssq += a * a + b * b; }
      if ((u >> 4) == h) *(v4u*)(memall + ((size_t)s * NMEM + (u & 7)) * DM + (u >> 3) * 8) = mu[i];
    }
#pragma unroll
    for (int o = 32; o; o >>= 1) ssq += __shfl_xor(ssq, o);
    if (lane == 0) *(float*)(smem + REC_LDS_NRM + w * 4) = ssq;
    __syncthreads();
    if (s == NSEG - 1) break;
    f32x4 aq[2] = {}, ak[2] = {}, av[2] = {};
    const unsigned char* mfp = smem + REC_LDS_MEM + (g * 8 + (fr & 7)) * 16;
#pragma unroll
    for (int kk = 0; kk < 16; ++kk) {
      v4u mf = *(const v4u*)(mfp + kk * 512);
      aq[0] = mfma16(wq[0][kk], mf, aq[0]);
      aq[1] = mfma16(wq[1][kk], mf, aq[1]);
      ak[0] = mfma16(wk[0][kk], mf, ak[0]);
      ak[1] = mfma16(wk[1][kk], mf, ak[1]);
    }
    v4u qf = {pack2(aq[0][0], aq[0][1]), pack2(aq[0][2], aq[0][3]), pack2(aq[1][0], aq[1][1]), pack2(aq[1][2], aq[1][3])};
    v4u kf = {pack2(ak[0][0], ak[0][1]), pack2(ak[0][2], ak[0][3]), pack2(ak[1][0], ak[1][1]), pack2(ak[1][2], ak[1][3])};
    f32x4 z4 = {0.f, 0.f, 0.f, 0.f};
    f32x4 sm = mfma16(kf, qf, z4), s0 = mfma16(kx[0], qf, z4), s1 = mfma16(kx[1], qf, z4);
    *(f32x4*)(smem + REC_LDS_SC + ((w * 3 + 0) * 64 + lane) * 16) = sm;
    *(f32x4*)(smem + REC_LDS_SC + ((w * 3 + 1) * 64 + lane) * 16) = s0;
    *(f32x4*)(smem + REC_LDS_SC + ((w * 3 + 2) * 64 + lane) * 16) = s1;
    __syncthreads();
#pragma unroll
    for (int kk = 0; kk < 16; ++kk) {
      v4u b0 = *(const v4u*)(smem + REC_LDS_WV + (((w * 2 + 0) * 16 + kk) * 64 + lane) * 16);
      v4u b1 = *(const v4u*)(smem + REC_LDS_WV + (((w * 2 + 1) * 16 + kk) * 64 + lane) * 16);
      v4u mf = *(const v4u*)(mfp + kk * 512);
      av[0] = mfma16(mf, b0, av[0]);
      av[1] = mfma16(mf, b1, av[1]);
    }
    float sc[12];
#pragma unroll
    for (int t = 0; t < 3; ++t) {
      f32x4 a = *(const f32x4*)(smem + REC_LDS_SC + ((0 * 3 + t) * 64 + lane) * 16);
#pragma unroll
      for (int ww = 1; ww < 4; ++ww) { f32x4 b = *(const f32x4*)(smem + REC_LDS_SC + ((ww * 3 + t) * 64 + lane) * 16); a += b; }
#pragma unroll
      for (int j = 0; j < 4; ++j) sc[4 * t + j] = rbf(rbf(a[j]) * scale);
    }
    if (g >= 2) { sc[0] = sc[1] = sc[2] = sc[3] = -INFINITY; }
    float mx = sc[0];
#pragma unroll
    for (int j = 1; j < 12; ++j) mx = fmaxf(mx, sc[j]);
    mx = fmaxf(mx, __shfl_xor(mx, 16));
    mx = fmaxf(mx, __shfl_xor(mx, 32));
    float sum = 0.f;
#pragma unroll
    for (int j = 0; j < 12; ++j) { sc[j] = __expf(sc[j] - mx); sum += sc[j]; }
    sum += __shfl_xor(sum, 16);
    sum += __shfl_xor(sum, 32);
    float inv = 1.f / sum;
    v4u pm = {pack2(sc[0] * inv, sc[1] * inv), pack2(sc[2] * inv, sc[3] * inv), 0u, 0u};
    v4u px = {pack2(sc[4] * inv, sc[5] * inv), pack2(sc[6] * inv, sc[7] * inv), pack2(sc[8] * inv, sc[9] * inv), pack2(sc[10] * inv, sc[11] * inv)};
    f32x4 ao[2];
#pragma unroll
    for (int t = 0; t < 2; ++t) {
      v4u vm = {pack2(av[t][0], av[t][1]), pack2(av[t][2], av[t][3]), 0u, 0u};
      ao[t] = mfma16(vx[t], px, z4);
      ao[t] = mfma16(vm, pm, ao[t]);
    }
    v4u of = {pack2(ao[0][0], ao[0][1]), pack2(ao[0][2], ao[0][3]), pack2(ao[1][0], ao[1][1]), pack2(ao[1][2], ao[1][3])};
    *(v4u*)(smem + REC_LDS_O + (w * 64 + lane) * 16) = of;
    prefetch(s + 1);
    f32x4 nr = *(const f32x4*)(smem + REC_LDS_NRM);
    float nrm = fmaxf(sqrtf(nr[0] + nr[1] + nr[2] + nr[3]) * 1.001f, 23.0f);
    float bound = nrm * wbound;
    unsigned eb = (__float_as_uint(bound) >> 23) & 0xFFu;
    int de = (int)eb + 1 - 23;  if (de < 1) de = 1; if (de > 254) de = 254;
    float delta = __uint_as_float((unsigned)de << 23);
    float rdelta = __uint_as_float((unsigned)(254 - de) << 23);
    __syncthreads();
    v4u ofr[4];
#pragma unroll
    for (int ks = 0; ks < 4; ++ks) ofr[ks] = *(const v4u*)(smem + REC_LDS_O + (ks * 64 + lane) * 16);
    u64* adst = acc + (size_t)(s + 1) * 2048 + ((g >> 1) * 8 + (fr & 7)) * 4 + (g & 1) * 2 + (fr >> 3);
#pragma unroll
    for (int t = 0; t < 8; ++t) {
      f32x4 a = z4;
#pragma unroll
      for (int ks = 0; ks < 4; ++ks) a = mfma16(wo[t][ks], ofr[ks], a);
      float x0 = (fr >> 3) ? a[2] : a[0], x1 = (fr >> 3) ? a[3] : a[1];
      float r0 = fminf(fmaxf(rintf(x0 * rdelta), -8388608.f), 8388607.f);
      float r1 = fminf(fmaxf(rintf(x1 * rdelta), -8388608.f), 8388607.f);
      unsigned i0 = (unsigned)((int)r0 + 8388608), i1 = (unsigned)((int)r1 + 8388608);
      u64 v = (u64)i0 | ((u64)i1 << 29) | (1ull << 58);
      __hip_atomic_fetch_add(adst + (8 * w + t) * 64, v, __ATOMIC_RELAXED, __HIP_MEMORY_SCOPE_AGENT);
    }
    delta_prev = delta;
  }
}

__global__ __launch_bounds__(256) void k_attn(const u16* __restrict__ Q, const u16* __restrict__ Kx, const u16* __restrict__ VT,
                                              const u16* __restrict__ KM, const u16* __restrict__ VMT, u16* __restrict__ O) {
  const int lane = threadIdx.x & 63, wv = threadIdx.x >> 6;
  const int task = blockIdx.x * 4 + wv;
  const int s = task >> 5, h = task & 31;
  const int fr = lane & 15, g = lane >> 4;
  const float scale = 0.08838834764831845f;
  f32x4 sc[3][2] = {};
#pragma unroll
  for (int ks = 0; ks < 4; ++ks) {
    v4u qf[2], kf[3];
#pragma unroll
    for (int t = 0; t < 2; ++t) {
      qf[t] = *(const v4u*)(Q + (size_t)(s * SEG + 16 * t + fr) * HD + h * DH + 32 * ks + 8 * g);
      kf[1 + t] = *(const v4u*)(Kx + (size_t)(s * SEG + 16 * t + fr) * HD + h * DH + 32 * ks + 8 * g);
    }
    kf[0] = *(const v4u*)(KM + (size_t)(s * NMEM + (fr & 7)) * HD + h * DH + 32 * ks + 8 * g);
#pragma unroll
    for (int a = 0; a < 3; ++a)
#pragma unroll
      for (int t = 0; t < 2; ++t) sc[a][t] = mfma16(kf[a], qf[t], sc[a][t]);
  }
  v4u pm[2], px[2];
#pragma unroll
  for (int t = 0; t < 2; ++t) {
    const int qi = 16 * t + fr;
    float v[12];
#pragma unroll
    for (int a = 0; a < 3; ++a)
#pragma unroll
      for (int j = 0; j < 4; ++j) {
        float x = rbf(rbf(sc[a][t][j]) * scale);
        bool valid;
        if (a == 0) valid = g < 2;
        else valid = (16 * (a - 1) + 4 * g + j) <= qi;
        v[4 * a + j] = valid ? x : -INFINITY;
      }
    float mx = v[0];
#pragma unroll
    for (int j = 1; j < 12; ++j) mx = fmaxf(mx, v[j]);
    mx = fmaxf(mx, __shfl_xor(mx, 16));
    mx = fmaxf(mx, __shfl_xor(mx, 32));
    float sum = 0.f;
#pragma unroll
    for (int j = 0; j < 12; ++j) { v[j] = __expf(v[j] - mx); sum += v[j]; }
    sum += __shfl_xor(sum, 16);
    sum += __shfl_xor(sum, 32);
    float inv = 1.f / sum;
    pm[t] = (v4u){pack2(v[0] * inv, v[1] * inv), pack2(v[2] * inv, v[3] * inv), 0u, 0u};
    px[t] = (v4u){pack2(v[4] * inv, v[5] * inv), pack2(v[6] * inv, v[7] * inv), pack2(v[8] * inv, v[9] * inv), pack2(v[10] * inv, v[11] * inv)};
  }
#pragma unroll
  for (int et = 0; et < 8; ++et) {
    const u16* q = VT + ((size_t)s * HD + h * DH + 16 * et + fr) * 32 + 4 * g;
    v2u c = *(const v2u*)q, d = *(const v2u*)(q + 16);
    v4u vx = {c.x, c.y, d.x, d.y};
    v2u m = *(const v2u*)(VMT + ((size_t)s * HD + h * DH + 16 * et + fr) * 8 + 4 * (g & 1));
    v4u vm = {m.x, m.y, 0u, 0u};
#pragma unroll
    for (int t = 0; t < 2; ++t) {
      f32x4 a = {0.f, 0.f, 0.f, 0.f};
      a = mfma16(vx, px[t], a);
      a = mfma16(vm, pm[t], a);
      v2u o = {pack2(a[0], a[1]), pack2(a[2], a[3])};
      *(v2u*)(O + (size_t)(s * SEG + 16 * t + fr) * HD + h * DH + 16 * et + 4 * g) = o;
    }
  }
}

__device__ __forceinline__ float phi(float t) { return rbf((t > 0.f ? rbf(t + 1.f) : rbf(__expf(t))) + 1e-6f); }

__global__ __launch_bounds__(512) void k_kvpart(const u16* __restrict__ qkv, float* __restrict__ kvp) {
  const int blk = blockIdx.x, t = threadIdx.x, h = t >> 4, d = t & 15;
  float acc[16] = {}, z = 0.f;
  for (int i = 0; i < 32; ++i) {
    const u16* row = qkv + (size_t)(blk * 32 + i) * 1536;
    float pk = phi(bf2f(row[512 + t]));
    v4u v0 = *(const v4u*)(row + 1024 + h * 16), v1 = *(const v4u*)(row + 1024 + h * 16 + 8);
    const u16* e0 = (const u16*)&v0; const u16* e1 = (const u16*)&v1;
#pragma unroll
    for (int j = 0; j < 8; ++j) { acc[j] += pk * bf2f(e0[j]); acc[8 + j] += pk * bf2f(e1[j]); }
    z += pk;
  }
  float* o = kvp + ((size_t)blk * 32 + h) * 272;
#pragma unroll
  for (int j = 0; j < 16; ++j) o[d * 16 + j] = acc[j];
  o[256 + d] = z;
}

__global__ __launch_bounds__(272) void k_kvred(const float* __restrict__ kvp, float* __restrict__ kv) {
  const int h = blockIdx.x, t = threadIdx.x;
  float s = 0.f;
  for (int b = 0; b < 64; ++b) s += kvp[((size_t)b * 32 + h) * 272 + t];
  kv[h * 272 + t] = rbf(s);
}

__global__ __launch_bounds__(512) void k_linout(const u16* __restrict__ qkv, const float* __restrict__ kv, u16* __restrict__ ob) {
  __shared__ float skv[32 * 272];
  const int t = threadIdx.x, h = t >> 4, e = t & 15;
  for (int i = t; i < 32 * 272; i += 512) skv[i] = kv[i];
  __syncthreads();
  for (int i = 0; i < 8; ++i) {
    int tok = blockIdx.x * 8 + i;
    const u16* row = qkv + (size_t)tok * 1536 + h * 16;
    v4u v0 = *(const v4u*)(row), v1 = *(const v4u*)(row + 8);
    const u16* e0 = (const u16*)&v0; const u16* e1 = (const u16*)&v1;
    float num = 0.f, den = 0.f;
#pragma unroll
    for (int d = 0; d < 16; ++d) {
      float pq = phi(bf2f(d < 8 ? e0[d] : e1[d - 8]));
      num += pq * skv[h * 272 + d * 16 + e];
      den += pq * skv[h * 272 + 256 + d];
    }
    float dn = rbf(rbf(den) + 1e-6f);
    ob[(size_t)tok * DM + t] = f2bf(rbf(num) / dn);
  }
}

extern "C" void kernel_launch(void* const* d_in, const int* in_sizes, int n_in, void* d_out, int out_size, void* d_ws, size_t ws_size,
                              hipStream_t stream) {
  const u16* A_x = (const u16*)d_in[0];
  const u16* A_mem0 = (const u16*)d_in[1];
  const u16* A_Wq = (const u16*)d_in[2];
  const u16* A_Wk = (const u16*)d_in[3];
  const u16* A_Wv = (const u16*)d_in[4];
  const u16* A_Wo = (const u16*)d_in[5];
  const u16* B_wqkv = (const u16*)d_in[8];
  const u16* B_wo = (const u16*)d_in[9];
  unsigned char* ws = (unsigned char*)d_ws;
  u64* acc = (u64*)(ws + OFF_ACC);
  unsigned* ctl = (unsigned*)(ws + OFF_CTL);
  u16* WT = (u16*)(ws + OFF_WT);
  u16* WoT = (u16*)(ws + OFF_WOT);
  u16* BqT = (u16*)(ws + OFF_BQKVT);
  u16* BoT = (u16*)(ws + OFF_BOT);
  u16* xn = (u16*)(ws + OFF_XN);
  u16* xc = (u16*)(ws + OFF_XCUR);
  u16* Qb = (u16*)(ws + OFF_Q);
  u16* Kb = (u16*)(ws + OFF_K);
  u16* VT = (u16*)(ws + OFF_VT);
  u16* memall = (u16*)(ws + OFF_MEM);
  u16* KM = (u16*)(ws + OFF_KM);
  u16* VMT = (u16*)(ws + OFF_VMT);
  u16* Ob = (u16*)(ws + OFF_O);
  float* part = (float*)(ws + OFF_PART);
  u16* qkvB = (u16*)(ws + OFF_QKVB);
  float* kvp = (float*)(ws + OFF_KVP);
  float* kv = (float*)(ws + OFF_KV);
  u16* ob = (u16*)(ws + OFF_OB);

  hipFuncSetAttribute((const void*)k_rec, hipFuncAttributeMaxDynamicSharedMemorySize, REC_LDS_BYTES);
  hipMemsetAsync(ws, 0, SZ_ACC + SZ_CTL, stream);
  k_transpose<<<2304, 256, 0, stream>>>(A_Wq, A_Wk, A_Wv, A_Wo, B_wqkv, B_wo, WT, WoT, BqT, BoT);
  k_bounds<<<32, 512, 0, stream>>>(A_Wv, A_Wo, ctl);
  const u16* xsrc = A_x;
  for (int layer = 0; layer < 2; ++layer) {
    if (layer == 0) k_ln<0, true><<<512, 256, 0, stream>>>(xsrc, nullptr, nullptr, xn);
    else k_ln<1, true><<<512, 256, 0, stream>>>(xc, part, xc, xn);
    k_gemm<2><<<dim3(96, 16, 1), 256, 65536, stream>>>(xn, WT, S_TOK, 3 * HD, DM, DM, Qb, Kb, VT, nullptr, 0);
    k_rec<<<32, 256, REC_LDS_BYTES, stream>>>(WT, WoT, A_mem0, Kb, VT, acc + (size_t)layer * NSEG * 2048, memall, ctl);
    k_gemm<3><<<dim3(64, 4, 1), 256, 65536, stream>>>(memall, WT + (size_t)HD * DM, NSEG * NMEM, 2 * HD, DM, DM, KM, VMT, nullptr, nullptr, 0);
    k_attn<<<512, 256, 0, stream>>>(Qb, Kb, VT, KM, VMT, Ob);
    k_gemm<1><<<dim3(4, 16, 4), 256, 65536, stream>>>(Ob, WoT, S_TOK, DM, HD, HD / 4, nullptr, nullptr, nullptr, part, 0);
    k_ln<4, true><<<512, 256, 0, stream>>>(layer == 0 ? xsrc : xc, part, xc, xn);
    k_gemm<0><<<dim3(12, 16, 1), 256, 65536, stream>>>(xn, BqT, S_TOK, 3 * DM, DM, DM, qkvB, nullptr, nullptr, nullptr, 3 * DM);
    k_kvpart<<<64, 512, 0, stream>>>(qkvB, kvp);
    k_kvred<<<32, 272, 0, stream>>>(kvp, kv);
    k_linout<<<256, 512, 0, stream>>>(qkvB, kv, ob);
    k_gemm<1><<<dim3(4, 16, 1), 256, 65536, stream>>>(ob, BoT, S_TOK, DM, DM, DM, nullptr, nullptr, nullptr, part, 0);
  }
  k_ln<1, false><<<512, 256, 0, stream>>>(xc, part, (u16*)d_out, nullptr);
}
```

```cpp
#define NOLNF
#define S2ST 64
#define EPX 0
#define CPF 136
#include <hip/hip_runtime.h>
#include <hip/hip_bf16.h>
#include <stdint.h>

typedef unsigned short u16;
typedef unsigned long long u64;
typedef short bf16x8 __attribute__((ext_vector_type(8)));
typedef short bf16x4 __attribute__((ext_vector_type(4)));
typedef float f32x4 __attribute__((ext_vector_type(4)));
typedef float f32x2 __attribute__((ext_vector_type(2)));
typedef __bf16 bfv2 __attribute__((ext_vector_type(2)));
typedef unsigned v4u __attribute__((ext_vector_type(4)));
typedef unsigned v2u __attribute__((ext_vector_type(2)));
#define GAS __attribute__((address_space(1)))
#define LAS __attribute__((address_space(3)))
typedef GAS unsigned gu32;
#define RLX_AGENT __ATOMIC_RELAXED, __HIP_MEMORY_SCOPE_AGENT

constexpr int S_TOK = 2048, DM = 512, NH = 32, DH = 128, NMEM = 8, SEG = 32, NSEG = 64, HD = NH * DH;

__device__ __forceinline__ float bf2f(u16 b) { return __uint_as_float((unsigned)b << 16); }
__device__ __forceinline__ unsigned pack2(float a, float b) { f32x2 v = {a, b}; return __builtin_bit_cast(unsigned, __builtin_convertvector(v, bfv2)); }
__device__ __forceinline__ float rbf(float f) { return __uint_as_float(pack2(f, f) & 0xFFFF0000u); }
__device__ __forceinline__ float lo16(unsigned u) { return __uint_as_float(u << 16); }
__device__ __forceinline__ float hi16(unsigned u) { return __uint_as_float(u & 0xFFFF0000u); }
__device__ __forceinline__ f32x4 mfma16(v4u a, v4u b, f32x4 c) {
  return __builtin_amdgcn_mfma_f32_16x16x32_bf16(__builtin_bit_cast(bf16x8, a), __builtin_bit_cast(bf16x8, b), c, 0, 0, 0);
}
__device__ __forceinline__ f32x4 mfma16k(v2u a, v2u b, f32x4 c) {
  return __builtin_amdgcn_mfma_f32_16x16x16bf16_1k(__builtin_bit_cast(bf16x4, a), __builtin_bit_cast(bf16x4, b), c, 0, 0, 0);
}
__device__ __forceinline__ v2u pack4(f32x4 a) { return (v2u){pack2(a[0], a[1]), pack2(a[2], a[3])}; }
__device__ __forceinline__ v4u pack8(f32x4 a, f32x4 b) { return (v4u){pack2(a[0], a[1]), pack2(a[2], a[3]), pack2(b[0], b[1]), pack2(b[2], b[3])}; }

constexpr unsigned MB = 1u << 20;
constexpr unsigned OFF_ACC = 0;
constexpr unsigned OFF_CTL = 4 * MB;
constexpr unsigned OFF_FLG = OFF_CTL + 4096;
constexpr unsigned FL_QKV = 0;
constexpr unsigned FL_R = 131072;
constexpr unsigned FL_P = FL_R + 65536;
constexpr unsigned FL_B = FL_P + 8192;
constexpr unsigned FL_X = FL_B + 2048;
constexpr unsigned FL_KV = FL_X + 8192;
constexpr unsigned FL_K0 = FL_KV + 1024;
constexpr unsigned FL_PQ = FL_K0 + 1024;
constexpr unsigned FL_T = 229376;
constexpr unsigned FL_SIZE = 294912;
static_assert(FL_PQ + 8192 <= FL_T, "");
constexpr unsigned OFF_WT = 5 * MB;
constexpr unsigned OFF_HS = 9 * MB;
constexpr unsigned OFF_WOT = OFF_WT + 12 * MB;
constexpr unsigned OFF_WOF = OFF_WOT + 4 * MB;
constexpr unsigned OFF_BQT = OFF_WOF + 4 * MB;
constexpr unsigned OFF_WOBF = OFF_BQT + 2 * MB;
constexpr unsigned OFF_BT = OFF_WOBF + 1 * MB;
constexpr unsigned OFF_XNA = OFF_BT + 16 * MB;
constexpr unsigned OFF_XNB = OFF_XNA + 4 * MB;
constexpr unsigned OFF_X1 = OFF_XNB + 4 * MB;
constexpr unsigned OFF_X2 = OFF_X1 + 4 * MB;
constexpr unsigned OFF_PQ = OFF_X2 + 2 * MB;
constexpr unsigned OFF_KV0 = OFF_PQ + 4 * MB;
constexpr unsigned OFF_KVF = OFF_KV0 + 524288;
constexpr unsigned OFF_KF = OFF_KV0 + 1 * MB;
constexpr unsigned OFF_QF = OFF_KF + 32 * MB;
constexpr unsigned OFF_UF = OFF_QF + 16 * MB;
constexpr unsigned OFF_KM = OFF_UF + 64 * MB;
constexpr unsigned OFF_YF = OFF_KM + 4 * MB;
constexpr unsigned KY_L0 = 163 * MB;
static_assert(OFF_KM - KY_L0 == 12 * MB && OFF_YF - KY_L0 + 16 * MB <= OFF_XNA, "");
constexpr unsigned OFF_PART = OFF_YF + 16 * MB;
constexpr unsigned WS_END = OFF_PART + 32 * MB;
static_assert(WS_END <= 256 * MB, "");

__device__ __forceinline__ float wave_sum(float s) {
  s += __builtin_bit_cast(float, __builtin_amdgcn_update_dpp(0, __builtin_bit_cast(int, s), 0xB1, 0xF, 0xF, true));
  s += __builtin_bit_cast(float, __builtin_amdgcn_update_dpp(0, __builtin_bit_cast(int, s), 0x4E, 0xF, 0xF, true));
  s += __builtin_bit_cast(float, __builtin_amdgcn_update_dpp(0, __builtin_bit_cast(int, s), 0x128, 0xF, 0xF, true));
  s += __builtin_bit_cast(float, __builtin_amdgcn_update_dpp(0, __builtin_bit_cast(int, s), 0x124, 0xF, 0xF, true));
  { v2u r = __builtin_amdgcn_permlane16_swap(__float_as_uint(s), __float_as_uint(s), false, false); s = __uint_as_float(r.x) + __uint_as_float(r.y); }
  { v2u r = __builtin_amdgcn_permlane32_swap(__float_as_uint(s), __float_as_uint(s), false, false); s = __uint_as_float(r.x) + __uint_as_float(r.y); }
  return s;
}
__device__ __forceinline__ float x16_add(float s) { v2u r = __builtin_amdgcn_permlane16_swap(__float_as_uint(s), __float_as_uint(s), false, false); return __uint_as_float(r.x) + __uint_as_float(r.y); }
__device__ __forceinline__ float x32_add(float s) { v2u r = __builtin_amdgcn_permlane32_swap(__float_as_uint(s), __float_as_uint(s), false, false); return __uint_as_float(r.x) + __uint_as_float(r.y); }
__device__ __forceinline__ float x16_max(float s) { v2u r = __builtin_amdgcn_permlane16_swap(__float_as_uint(s), __float_as_uint(s), false, false); return fmaxf(__uint_as_float(r.x), __uint_as_float(r.y)); }
__device__ __forceinline__ float x32_max(float s) { v2u r = __builtin_amdgcn_permlane32_swap(__float_as_uint(s), __float_as_uint(s), false, false); return fmaxf(__uint_as_float(r.x), __uint_as_float(r.y)); }

__device__ __forceinline__ v4u ln_row(const float (&x)[8]) {
  float s = ((x[0] + x[1]) + (x[2] + x[3])) + ((x[4] + x[5]) + (x[6] + x[7]));
  s = wave_sum(s);
  const float mean = s * (1.f / DM);
  float q = 0.f;
#pragma unroll
  for (int j = 0; j < 8; ++j) { float d = x[j] - mean; q += d * d; }
  q = wave_sum(q);
  const float var = rbf(q * (1.f / DM));
  const float mb = rbf(mean);
  const float sq = rbf(sqrtf(rbf(var + 1e-5f)));
  const float r = 1.f / sq;
  unsigned d01 = pack2(x[0] - mb, x[1] - mb), d23 = pack2(x[2] - mb, x[3] - mb), d45 = pack2(x[4] - mb, x[5] - mb), d67 = pack2(x[6] - mb, x[7] - mb);
  return (v4u){pack2(lo16(d01) * r, hi16(d01) * r), pack2(lo16(d23) * r, hi16(d23) * r), pack2(lo16(d45) * r, hi16(d45) * r), pack2(lo16(d67) * r, hi16(d67) * r)};
}

#define REC_ASM_BODY \
  "s_nop 3\n" \
  "s_mov_b32 s76, %[w]\n" \
  "s_mov_b64 s[36:37], %[ws]\n" \
  "s_mov_b32 s38, %[wsz]\n" \
  "s_mov_b32 s39, 0x00020000\n" \
  "s_mov_b32 s42, 0x400000\n" \
  "s_mov_b32 s43, 0x00020000\n" \
  "v_mbcnt_lo_u32_b32 v8, -1, 0\n" \
  "v_mbcnt_hi_u32_b32 v8, -1, v8\n" \
  "v_lshlrev_b32 v8, 4, v8\n" \
  "s_lshl_b32 s58, s76, 12\n" \
  "s_add_u32 s58, s58, %[oWarm]\n" \
  "s_add_u32 s58, s58, 5242880\n" \
  "buffer_store_dwordx4 v[0:3], v8, s[36:39], s58 offen offset:0\n" \
  "buffer_store_dwordx4 v[4:7], v8, s[36:39], s58 offen offset:1024\n" \
  "v_lshlrev_b32 v136, 6, v8\n" \
  "v_lshlrev_b32 v140, 8, v8\n" \
  "v_mov_b32 v139, 1\n" \
  "s_lshr_b32 s51, %[oWarm], 6\n" \
  "s_and_b32 s51, s51, 768\n" \
  "s_add_u32 s51, s51, 9437184\n" \
  "s_cmp_eq_u32 s76, 0\n" \
  "s_cselect_b32 s66, 255, 0\n" \
  "s_mov_b32 s67, 0\n" \
  "s_mov_b64 exec, s[66:67]\n" \
  "buffer_load_dword v137, v136, s[36:39], s51 offen offset:0 sc1\n" \
  "s_mov_b64 exec, -1\n" \
  "v_mbcnt_lo_u32_b32 v0, -1, 0\n" \
  "v_mbcnt_hi_u32_b32 v0, -1, v0\n" \
  "v_and_b32 v1, 15, v0\n" \
  "v_lshrrev_b32 v2, 4, v0\n" \
  "v_lshrrev_b32 v3, 2, v1\n" \
  "v_and_b32 v4, 3, v1\n" \
  "v_lshlrev_b32 v5, 1, v2\n" \
  "v_xor_b32 v5, v5, v1\n" \
  "v_lshlrev_b32 v5, 4, v5\n" \
  "v_xor_b32 v6, 128, v5\n" \
  "v_lshl_add_u32 v8, v2, 13, v5\n" \
  "v_lshl_add_u32 v9, v2, 13, v6\n" \
  "v_lshl_add_u32 v10, v2, 10, v5\n" \
  "v_lshl_add_u32 v11, v2, 10, v6\n" \
  "s_lshl_b32 s45, s76, 14\n" \
  "s_and_b32 s52, %[h], 7\n" \
  "s_lshl_b32 s52, s52, 2\n" \
  "s_lshr_b32 s58, %[h], 3\n" \
  "s_add_u32 s52, s52, s58\n" \
  "s_lshl_b32 s46, s76, 20\n" \
  "s_lshl_b32 s58, s52, 8\n" \
  "s_add_u32 s46, s46, s58\n" \
  "s_mov_b64 s[40:41], %[pWo]\n" \
  "s_lshl_b32 s59, s52, 17\n" \
  "s_lshl_b32 s58, s76, 15\n" \
  "s_add_u32 s59, s59, s58\n" \
  "s_lshl_b32 s58, %[cq], 8\n" \
  "s_add_u32 s59, s59, s58\n" \
  "s_lshr_b32 s60, s45, 1\n" \
  "s_add_u32 m0, s60, 131072\n" \
  "s_add_u32 s58, s59, 0\n" \
  "buffer_load_dwordx4 v10, s[40:43], s58 offen lds\n" \
  "s_add_u32 m0, s60, 132096\n" \
  "s_add_u32 s58, s59, 4096\n" \
  "buffer_load_dwordx4 v10, s[40:43], s58 offen lds\n" \
  "s_add_u32 m0, s60, 133120\n" \
  "s_add_u32 s58, s59, 8192\n" \
  "buffer_load_dwordx4 v11, s[40:43], s58 offen lds\n" \
  "s_add_u32 m0, s60, 134144\n" \
  "s_add_u32 s58, s59, 12288\n" \
  "buffer_load_dwordx4 v11, s[40:43], s58 offen lds\n" \
  "s_add_u32 m0, s60, 135168\n" \
  "s_add_u32 s58, s59, 16384\n" \
  "buffer_load_dwordx4 v10, s[40:43], s58 offen lds\n" \
  "s_add_u32 m0, s60, 136192\n" \
  "s_add_u32 s58, s59, 20480\n" \
  "buffer_load_dwordx4 v10, s[40:43], s58 offen lds\n" \
  "s_add_u32 m0, s60, 137216\n" \
  "s_add_u32 s58, s59, 24576\n" \
  "buffer_load_dwordx4 v11, s[40:43], s58 offen lds\n" \
  "s_add_u32 m0, s60, 138240\n" \
  "s_add_u32 s58, s59, 28672\n" \
  "buffer_load_dwordx4 v11, s[40:43], s58 offen lds\n" \
  "s_mov_b64 s[40:41], %[pWv]\n" \
  "s_add_u32 m0, s45, 0\n" \
  "s_add_u32 s58, s46, 0\n" \
  "buffer_load_dwordx4 v8, s[40:43], s58 offen lds\n" \
  "s_add_u32 m0, s45, 1024\n" \
  "s_add_u32 s58, s46, 32768\n" \
  "buffer_load_dwordx4 v8, s[40:43], s58 offen lds\n" \
  "s_add_u32 m0, s45, 2048\n" \
  "s_add_u32 s58, s46, 65536\n" \
  "buffer_load_dwordx4 v9, s[40:43], s58 offen lds\n" \
  "s_add_u32 m0, s45, 3072\n" \
  "s_add_u32 s58, s46, 98304\n" \
  "buffer_load_dwordx4 v9, s[40:43], s58 offen lds\n" \
  "s_add_u32 m0, s45, 4096\n" \
  "s_add_u32 s58, s46, 131072\n" \
  "buffer_load_dwordx4 v8, s[40:43], s58 offen lds\n" \
  "s_add_u32 m0, s45, 5120\n" \
  "s_add_u32 s58, s46, 163840\n" \
  "buffer_load_dwordx4 v8, s[40:43], s58 offen lds\n" \
  "s_add_u32 m0, s45, 6144\n" \
  "s_add_u32 s58, s46, 196608\n" \
  "buffer_load_dwordx4 v9, s[40:43], s58 offen lds\n" \
  "s_add_u32 m0, s45, 7168\n" \
  "s_add_u32 s58, s46, 229376\n" \
  "buffer_load_dwordx4 v9, s[40:43], s58 offen lds\n" \
  "s_add_u32 m0, s45, 8192\n" \
  "s_add_u32 s58, s46, 262144\n" \
  "buffer_load_dwordx4 v8, s[40:43], s58 offen lds\n" \
  "s_add_u32 m0, s45, 9216\n" \
  "s_add_u32 s58, s46, 294912\n" \
  "buffer_load_dwordx4 v8, s[40:43], s58 offen lds\n" \
  "s_add_u32 m0, s45, 10240\n" \
  "s_add_u32 s58, s46, 327680\n" \
  "buffer_load_dwordx4 v9, s[40:43], s58 offen lds\n" \
  "s_add_u32 m0, s45, 11264\n" \
  "s_add_u32 s58, s46, 360448\n" \
  "buffer_load_dwordx4 v9, s[40:43], s58 offen lds\n" \
  "s_add_u32 m0, s45, 12288\n" \
  "s_add_u32 s58, s46, 393216\n" \
  "buffer_load_dwordx4 v8, s[40:43], s58 offen lds\n" \
  "s_add_u32 m0, s45, 13312\n" \
  "s_add_u32 s58, s46, 425984\n" \
  "buffer_load_dwordx4 v8, s[40:43], s58 offen lds\n" \
  "s_add_u32 m0, s45, 14336\n" \
  "s_add_u32 s58, s46, 458752\n" \
  "buffer_load_dwordx4 v9, s[40:43], s58 offen lds\n" \
  "s_add_u32 m0, s45, 15360\n" \
  "s_add_u32 s58, s46, 491520\n" \
  "buffer_load_dwordx4 v9, s[40:43], s58 offen lds\n" \
  "s_mov_b64 s[40:41], %[pWq]\n" \
  "s_add_u32 m0, s45, 65536\n" \
  "s_add_u32 s58, s46, 0\n" \
  "buffer_load_dwordx4 v8, s[40:43], s58 offen lds\n" \
  "s_add_u32 m0, s45, 66560\n" \
  "s_add_u32 s58, s46, 32768\n" \
  "buffer_load_dwordx4 v8, s[40:43], s58 offen lds\n" \
  "s_add_u32 m0, s45, 67584\n" \
  "s_add_u32 s58, s46, 65536\n" \
  "buffer_load_dwordx4 v9, s[40:43], s58 offen lds\n" \
  "s_add_u32 m0, s45, 68608\n" \
  "s_add_u32 s58, s46, 98304\n" \
  "buffer_load_dwordx4 v9, s[40:43], s58 offen lds\n" \
  "s_add_u32 m0, s45, 69632\n" \
  "s_add_u32 s58, s46, 131072\n" \
  "buffer_load_dwordx4 v8, s[40:43], s58 offen lds\n" \
  "s_add_u32 m0, s45, 70656\n" \
  "s_add_u32 s58, s46, 163840\n" \
  "buffer_load_dwordx4 v8, s[40:43], s58 offen lds\n" \
  "s_add_u32 m0, s45, 71680\n" \
  "s_add_u32 s58, s46, 196608\n" \
  "buffer_load_dwordx4 v9, s[40:43], s58 offen lds\n" \
  "s_add_u32 m0, s45, 72704\n" \
  "s_add_u32 s58, s46, 229376\n" \
  "buffer_load_dwordx4 v9, s[40:43], s58 offen lds\n" \
  "s_add_u32 m0, s45, 73728\n" \
  "s_add_u32 s58, s46, 262144\n" \
  "buffer_load_dwordx4 v8, s[40:43], s58 offen lds\n" \
  "s_add_u32 m0, s45, 74752\n" \
  "s_add_u32 s58, s46, 294912\n" \
  "buffer_load_dwordx4 v8, s[40:43], s58 offen lds\n" \
  "s_add_u32 m0, s45, 75776\n" \
  "s_add_u32 s58, s46, 327680\n" \
  "buffer_load_dwordx4 v9, s[40:43], s58 offen lds\n" \
  "s_add_u32 m0, s45, 76800\n" \
  "s_add_u32 s58, s46, 360448\n" \
  "buffer_load_dwordx4 v9, s[40:43], s58 offen lds\n" \
  "s_add_u32 m0, s45, 77824\n" \
  "s_add_u32 s58, s46, 393216\n" \
  "buffer_load_dwordx4 v8, s[40:43], s58 offen lds\n" \
  "s_add_u32 m0, s45, 78848\n" \
  "s_add_u32 s58, s46, 425984\n" \
  "buffer_load_dwordx4 v8, s[40:43], s58 offen lds\n" \
  "s_add_u32 m0, s45, 79872\n" \
  "s_add_u32 s58, s46, 458752\n" \
  "buffer_load_dwordx4 v9, s[40:43], s58 offen lds\n" \
  "s_add_u32 m0, s45, 80896\n" \
  "s_add_u32 s58, s46, 491520\n" \
  "buffer_load_dwordx4 v9, s[40:43], s58 offen lds\n" \
  "v_mov_b32 v100, 0x88000000\n" \
  "v_mov_b32 v101, 0x87fffff7\n" \
  "v_mov_b32 v102, 0x88000000\n" \
  "v_mov_b32 v103, 0x87fffff7\n" \
  "v_lshlrev_b32 v104, 4, v0\n" \
  "s_lshl_b32 s58, s76, 12\n" \
  "s_add_u32 s58, s58, %[oWarm]\n" \
  "s_add_u32 s59, s58, 2097152\n" \
  "buffer_store_dwordx4 v[100:103], v104, s[36:39], s58 offen offset:0 sc1\n" \
  "buffer_store_dwordx4 v[100:103], v104, s[36:39], s58 offen offset:1024 sc1\n" \
  "buffer_store_dwordx4 v[100:103], v104, s[36:39], s58 offen offset:2048 sc1\n" \
  "buffer_store_dwordx4 v[100:103], v104, s[36:39], s58 offen offset:3072 sc1\n" \
  "buffer_store_dwordx4 v[100:103], v104, s[36:39], s59 offen offset:0 sc1\n" \
  "buffer_store_dwordx4 v[100:103], v104, s[36:39], s59 offen offset:1024 sc1\n" \
  "buffer_store_dwordx4 v[100:103], v104, s[36:39], s59 offen offset:2048 sc1\n" \
  "buffer_store_dwordx4 v[100:103], v104, s[36:39], s59 offen offset:3072 sc1\n" \
  "s_add_u32 s47, s76, 0\n" \
  "s_and_b32 s47, s47, 3\n" \
  "s_add_u32 s48, s76, 1\n" \
  "s_and_b32 s48, s48, 3\n" \
  "s_add_u32 s49, s76, 2\n" \
  "s_and_b32 s49, s49, 3\n" \
  "s_add_u32 s50, s76, 3\n" \
  "s_and_b32 s50, s50, 3\n" \
  "v_bfe_u32 v5, v1, 1, 1\n" \
  "v_and_b32 v6, 1, v1\n" \
  "v_lshl_add_u32 v5, v5, 3, v6\n" \
  "v_lshl_add_u32 v5, v3, 1, v5\n" \
  "v_lshl_add_u32 v5, v2, 4, v5\n" \
  "v_lshlrev_b32 v12, 2, v5\n" \
  "v_mov_b32 v13, 0\n" \
  "v_and_b32 v5, 1, v2\n" \
  "v_lshl_add_u32 v5, v5, 2, v3\n" \
  "v_lshlrev_b32 v5, 1, v5\n" \
  "v_lshrrev_b32 v6, 1, v4\n" \
  "s_lshl_b32 s58, s76, 2\n" \
  "v_add_u32 v6, s58, v6\n" \
  "v_lshlrev_b32 v7, 11, v2\n" \
  "v_lshl_add_u32 v7, v3, 8, v7\n" \
  "v_and_b32 v14, 1, v4\n" \
  "v_lshl_add_u32 v7, v14, 3, v7\n" \
  "v_add_u32 v14, 0, v6\n" \
  "v_xor_b32 v14, v14, v5\n" \
  "v_lshl_add_u32 v14, v14, 4, v7\n" \
  "v_add_u32 v15, 2, v6\n" \
  "v_xor_b32 v15, v15, v5\n" \
  "v_lshl_add_u32 v15, v15, 4, v7\n" \
  "s_lshl_b32 s58, s47, 14\n" \
  "v_add_u32 v16, s58, v14\n" \
  "v_add_u32 v24, 65536, v16\n" \
  "s_lshl_b32 s58, s47, 13\n" \
  "s_add_u32 s58, s58, 131072\n" \
  "v_add_u32 v80, s58, v14\n" \
  "s_lshl_b32 s58, s48, 14\n" \
  "v_add_u32 v17, s58, v14\n" \
  "v_add_u32 v25, 65536, v17\n" \
  "s_lshl_b32 s58, s48, 13\n" \
  "s_add_u32 s58, s58, 131072\n" \
  "v_add_u32 v81, s58, v14\n" \
  "s_lshl_b32 s58, s49, 14\n" \
  "v_add_u32 v18, s58, v14\n" \
  "v_add_u32 v26, 65536, v18\n" \
  "s_lshl_b32 s58, s49, 13\n" \
  "s_add_u32 s58, s58, 131072\n" \
  "v_add_u32 v82, s58, v14\n" \
  "s_lshl_b32 s58, s50, 14\n" \
  "v_add_u32 v19, s58, v14\n" \
  "v_add_u32 v27, 65536, v19\n" \
  "s_lshl_b32 s58, s50, 13\n" \
  "s_add_u32 s58, s58, 131072\n" \
  "v_add_u32 v83, s58, v14\n" \
  "s_lshl_b32 s58, s47, 14\n" \
  "v_add_u32 v20, s58, v15\n" \
  "v_add_u32 v28, 65536, v20\n" \
  "s_lshl_b32 s58, s47, 13\n" \
  "s_add_u32 s58, s58, 131072\n" \
  "v_add_u32 v84, s58, v15\n" \
  "s_lshl_b32 s58, s48, 14\n" \
  "v_add_u32 v21, s58, v15\n" \
  "v_add_u32 v29, 65536, v21\n" \
  "s_lshl_b32 s58, s48, 13\n" \
  "s_add_u32 s58, s58, 131072\n" \
  "v_add_u32 v85, s58, v15\n" \
  "s_lshl_b32 s58, s49, 14\n" \
  "v_add_u32 v22, s58, v15\n" \
  "v_add_u32 v30, 65536, v22\n" \
  "s_lshl_b32 s58, s49, 13\n" \
  "s_add_u32 s58, s58, 131072\n" \
  "v_add_u32 v86, s58, v15\n" \
  "s_lshl_b32 s58, s50, 14\n" \
  "v_add_u32 v23, s58, v15\n" \
  "v_add_u32 v31, 65536, v23\n" \
  "s_lshl_b32 s58, s50, 13\n" \
  "s_add_u32 s58, s58, 131072\n" \
  "v_add_u32 v87, s58, v15\n" \
  "v_and_b32 v5, 1, v3\n" \
  "v_lshl_add_u32 v5, v5, 2, v4\n" \
  "v_lshlrev_b32 v5, 1, v5\n" \
  "v_lshl_add_u32 v7, v3, 3, v4\n" \
  "v_lshlrev_b32 v7, 8, v7\n" \
  "s_lshl_b32 s58, s47, 2\n" \
  "v_add_u32 v6, s58, v2\n" \
  "v_xor_b32 v6, v6, v5\n" \
  "v_lshl_add_u32 v6, v6, 4, v7\n" \
  "s_lshl_b32 s58, s47, 14\n" \
  "v_add_u32 v32, s58, v6\n" \
  "s_lshl_b32 s58, s48, 14\n" \
  "v_add_u32 v36, s58, v6\n" \
  "s_lshl_b32 s58, s49, 14\n" \
  "v_add_u32 v40, s58, v6\n" \
  "s_lshl_b32 s58, s50, 14\n" \
  "v_add_u32 v44, s58, v6\n" \
  "s_lshl_b32 s58, s48, 2\n" \
  "v_add_u32 v6, s58, v2\n" \
  "v_xor_b32 v6, v6, v5\n" \
  "v_lshl_add_u32 v6, v6, 4, v7\n" \
  "s_lshl_b32 s58, s47, 14\n" \
  "v_add_u32 v33, s58, v6\n" \
  "s_lshl_b32 s58, s48, 14\n" \
  "v_add_u32 v37, s58, v6\n" \
  "s_lshl_b32 s58, s49, 14\n" \
  "v_add_u32 v41, s58, v6\n" \
  "s_lshl_b32 s58, s50, 14\n" \
  "v_add_u32 v45, s58, v6\n" \
  "s_lshl_b32 s58, s49, 2\n" \
  "v_add_u32 v6, s58, v2\n" \
  "v_xor_b32 v6, v6, v5\n" \
  "v_lshl_add_u32 v6, v6, 4, v7\n" \
  "s_lshl_b32 s58, s47, 14\n" \
  "v_add_u32 v34, s58, v6\n" \
  "s_lshl_b32 s58, s48, 14\n" \
  "v_add_u32 v38, s58, v6\n" \
  "s_lshl_b32 s58, s49, 14\n" \
  "v_add_u32 v42, s58, v6\n" \
  "s_lshl_b32 s58, s50, 14\n" \
  "v_add_u32 v46, s58, v6\n" \
  "s_lshl_b32 s58, s50, 2\n" \
  "v_add_u32 v6, s58, v2\n" \
  "v_xor_b32 v6, v6, v5\n" \
  "v_lshl_add_u32 v6, v6, 4, v7\n" \
  "s_lshl_b32 s58, s47, 14\n" \
  "v_add_u32 v35, s58, v6\n" \
  "s_lshl_b32 s58, s48, 14\n" \
  "v_add_u32 v39, s58, v6\n" \
  "s_lshl_b32 s58, s49, 14\n" \
  "v_add_u32 v43, s58, v6\n" \
  "s_lshl_b32 s58, s50, 14\n" \
  "v_add_u32 v47, s58, v6\n" \
  "s_waitcnt vmcnt(8)\n" \
  "s_waitcnt lgkmcnt(0)\n" \
  "s_barrier\n" \
  "ds_read_b64_tr_b16 a[0:1], v24 offset:0\n" \
  "ds_read_b64_tr_b16 a[2:3], v24 offset:1024\n" \
  "ds_read_b64_tr_b16 a[4:5], v24 offset:8192\n" \
  "ds_read_b64_tr_b16 a[6:7], v24 offset:9216\n" \
  "ds_read_b64_tr_b16 a[16:17], v25 offset:0\n" \
  "ds_read_b64_tr_b16 a[18:19], v25 offset:1024\n" \
  "ds_read_b64_tr_b16 a[20:21], v25 offset:8192\n" \
  "ds_read_b64_tr_b16 a[22:23], v25 offset:9216\n" \
  "ds_read_b64_tr_b16 a[32:33], v26 offset:0\n" \
  "ds_read_b64_tr_b16 a[34:35], v26 offset:1024\n" \
  "ds_read_b64_tr_b16 a[36:37], v26 offset:8192\n" \
  "ds_read_b64_tr_b16 a[38:39], v26 offset:9216\n" \
  "s_waitcnt lgkmcnt(4)\n" \
  "ds_read_b64_tr_b16 a[48:49], v27 offset:0\n" \
  "ds_read_b64_tr_b16 a[50:51], v27 offset:1024\n" \
  "ds_read_b64_tr_b16 a[52:53], v27 offset:8192\n" \
  "ds_read_b64_tr_b16 a[54:55], v27 offset:9216\n" \
  "ds_read_b64_tr_b16 a[64:65], v28 offset:0\n" \
  "ds_read_b64_tr_b16 a[66:67], v28 offset:1024\n" \
  "ds_read_b64_tr_b16 a[68:69], v28 offset:8192\n" \
  "ds_read_b64_tr_b16 a[70:71], v28 offset:9216\n" \
  "ds_read_b64_tr_b16 a[80:81], v29 offset:0\n" \
  "ds_read_b64_tr_b16 a[82:83], v29 offset:1024\n" \
  "ds_read_b64_tr_b16 a[84:85], v29 offset:8192\n" \
  "ds_read_b64_tr_b16 a[86:87], v29 offset:9216\n" \
  "s_waitcnt lgkmcnt(4)\n" \
  "ds_read_b64_tr_b16 a[96:97], v30 offset:0\n" \
  "ds_read_b64_tr_b16 a[98:99], v30 offset:1024\n" \
  "ds_read_b64_tr_b16 a[100:101], v30 offset:8192\n" \
  "ds_read_b64_tr_b16 a[102:103], v30 offset:9216\n" \
  "ds_read_b64_tr_b16 a[112:113], v31 offset:0\n" \
  "ds_read_b64_tr_b16 a[114:115], v31 offset:1024\n" \
  "ds_read_b64_tr_b16 a[116:117], v31 offset:8192\n" \
  "ds_read_b64_tr_b16 a[118:119], v31 offset:9216\n" \
  "s_waitcnt lgkmcnt(0)\n" \
  "ds_read_b64_tr_b16 v[48:49], v80 offset:0\n" \
  "ds_read_b64_tr_b16 v[50:51], v80 offset:1024\n" \
  "ds_read_b64_tr_b16 v[52:53], v81 offset:0\n" \
  "ds_read_b64_tr_b16 v[54:55], v81 offset:1024\n" \
  "ds_read_b64_tr_b16 v[56:57], v82 offset:0\n" \
  "ds_read_b64_tr_b16 v[58:59], v82 offset:1024\n" \
  "ds_read_b64_tr_b16 v[60:61], v83 offset:0\n" \
  "ds_read_b64_tr_b16 v[62:63], v83 offset:1024\n" \
  "s_waitcnt lgkmcnt(0)\n" \
  "ds_read_b64_tr_b16 v[64:65], v84 offset:0\n" \
  "ds_read_b64_tr_b16 v[66:67], v84 offset:1024\n" \
  "ds_read_b64_tr_b16 v[68:69], v85 offset:0\n" \
  "ds_read_b64_tr_b16 v[70:71], v85 offset:1024\n" \
  "ds_read_b64_tr_b16 v[72:73], v86 offset:0\n" \
  "ds_read_b64_tr_b16 v[74:75], v86 offset:1024\n" \
  "ds_read_b64_tr_b16 v[76:77], v87 offset:0\n" \
  "ds_read_b64_tr_b16 v[78:79], v87 offset:1024\n" \
  "s_waitcnt lgkmcnt(0)\n" \
  "s_waitcnt vmcnt(0)\n" \
  "s_waitcnt lgkmcnt(0)\n" \
  "s_barrier\n" \
  "s_mov_b64 s[40:41], %[pWv]\n" \
  "s_add_u32 m0, s45, 65536\n" \
  "s_add_u32 s58, s46, 524288\n" \
  "buffer_load_dwordx4 v8, s[40:43], s58 offen lds\n" \
  "s_add_u32 m0, s45, 66560\n" \
  "s_add_u32 s58, s46, 557056\n" \
  "buffer_load_dwordx4 v8, s[40:43], s58 offen lds\n" \
  "s_add_u32 m0, s45, 67584\n" \
  "s_add_u32 s58, s46, 589824\n" \
  "buffer_load_dwordx4 v9, s[40:43], s58 offen lds\n" \
  "s_add_u32 m0, s45, 68608\n" \
  "s_add_u32 s58, s46, 622592\n" \
  "buffer_load_dwordx4 v9, s[40:43], s58 offen lds\n" \
  "s_add_u32 m0, s45, 69632\n" \
  "s_add_u32 s58, s46, 655360\n" \
  "buffer_load_dwordx4 v8, s[40:43], s58 offen lds\n" \
  "s_add_u32 m0, s45, 70656\n" \
  "s_add_u32 s58, s46, 688128\n" \
  "buffer_load_dwordx4 v8, s[40:43], s58 offen lds\n" \
  "s_add_u32 m0, s45, 71680\n" \
  "s_add_u32 s58, s46, 720896\n" \
  "buffer_load_dwordx4 v9, s[40:43], s58 offen lds\n" \
  "s_add_u32 m0, s45, 72704\n" \
  "s_add_u32 s58, s46, 753664\n" \
  "buffer_load_dwordx4 v9, s[40:43], s58 offen lds\n" \
  "s_add_u32 m0, s45, 73728\n" \
  "s_add_u32 s58, s46, 786432\n" \
  "buffer_load_dwordx4 v8, s[40:43], s58 offen lds\n" \
  "s_add_u32 m0, s45, 74752\n" \
  "s_add_u32 s58, s46, 819200\n" \
  "buffer_load_dwordx4 v8, s[40:43], s58 offen lds\n" \
  "s_add_u32 m0, s45, 75776\n" \
  "s_add_u32 s58, s46, 851968\n" \
  "buffer_load_dwordx4 v9, s[40:43], s58 offen lds\n" \
  "s_add_u32 m0, s45, 76800\n" \
  "s_add_u32 s58, s46, 884736\n" \
  "buffer_load_dwordx4 v9, s[40:43], s58 offen lds\n" \
  "s_add_u32 m0, s45, 77824\n" \
  "s_add_u32 s58, s46, 917504\n" \
  "buffer_load_dwordx4 v8, s[40:43], s58 offen lds\n" \
  "s_add_u32 m0, s45, 78848\n" \
  "s_add_u32 s58, s46, 950272\n" \
  "buffer_load_dwordx4 v8, s[40:43], s58 offen lds\n" \
  "s_add_u32 m0, s45, 79872\n" \
  "s_add_u32 s58, s46, 983040\n" \
  "buffer_load_dwordx4 v9, s[40:43], s58 offen lds\n" \
  "s_add_u32 m0, s45, 80896\n" \
  "s_add_u32 s58, s46, 1015808\n" \
  "buffer_load_dwordx4 v9, s[40:43], s58 offen lds\n" \
  "v_lshrrev_b32 v100, 4, v0\n" \
  "v_lshlrev_b32 v100, 8, v100\n" \
  "v_and_b32 v101, 15, v0\n" \
  "v_lshl_add_u32 v100, v101, 3, v100\n" \
  "v_mov_b32 v102, 0\n" \
  "v_mov_b32 v103, 0\n" \
  "s_lshl_b32 s58, s76, 10\n" \
  "s_add_u32 s58, s58, %[oWarm]\n" \
  "s_add_u32 s58, s58, 0\n" \
  "v_add_u32 v100, s58, v100\n" \
  "global_atomic_add_x2 v100, v[102:103], s[36:37] offset:0\n" \
  "v_add_u32 v100, 4096, v100\n" \
  "global_atomic_add_x2 v100, v[102:103], s[36:37] offset:0\n" \
  "v_add_u32 v100, 4096, v100\n" \
  "global_atomic_add_x2 v100, v[102:103], s[36:37] offset:0\n" \
  "v_add_u32 v100, 4096, v100\n" \
  "global_atomic_add_x2 v100, v[102:103], s[36:37] offset:0\n" \
  "ds_bpermute_b32 v48, v12, v48\n" \
  "ds_bpermute_b32 v49, v12, v49\n" \
  "ds_bpermute_b32 v50, v12, v50\n" \
  "ds_bpermute_b32 v51, v12, v51\n" \
  "ds_bpermute_b32 v52, v12, v52\n" \
  "ds_bpermute_b32 v53, v12, v53\n" \
  "ds_bpermute_b32 v54, v12, v54\n" \
  "ds_bpermute_b32 v55, v12, v55\n" \
  "s_waitcnt lgkmcnt(0)\n" \
  "ds_bpermute_b32 v56, v12, v56\n" \
  "ds_bpermute_b32 v57, v12, v57\n" \
  "ds_bpermute_b32 v58, v12, v58\n" \
  "ds_bpermute_b32 v59, v12, v59\n" \
  "ds_bpermute_b32 v60, v12, v60\n" \
  "ds_bpermute_b32 v61, v12, v61\n" \
  "ds_bpermute_b32 v62, v12, v62\n" \
  "ds_bpermute_b32 v63, v12, v63\n" \
  "s_waitcnt lgkmcnt(0)\n" \
  "ds_bpermute_b32 v64, v12, v64\n" \
  "ds_bpermute_b32 v65, v12, v65\n" \
  "ds_bpermute_b32 v66, v12, v66\n" \
  "ds_bpermute_b32 v67, v12, v67\n" \
  "ds_bpermute_b32 v68, v12, v68\n" \
  "ds_bpermute_b32 v69, v12, v69\n" \
  "ds_bpermute_b32 v70, v12, v70\n" \
  "ds_bpermute_b32 v71, v12, v71\n" \
  "s_waitcnt lgkmcnt(0)\n" \
  "ds_bpermute_b32 v72, v12, v72\n" \
  "ds_bpermute_b32 v73, v12, v73\n" \
  "ds_bpermute_b32 v74, v12, v74\n" \
  "ds_bpermute_b32 v75, v12, v75\n" \
  "ds_bpermute_b32 v76, v12, v76\n" \
  "ds_bpermute_b32 v77, v12, v77\n" \
  "ds_bpermute_b32 v78, v12, v78\n" \
  "ds_bpermute_b32 v79, v12, v79\n" \
  "s_waitcnt lgkmcnt(0)\n" \
  "v_mfma_f32_16x16x32_bf16 a[144:147], v[48:51], v[48:51], 0\n" \
  "v_mfma_f32_16x16x32_bf16 a[144:147], v[52:55], v[52:55], a[144:147]\n" \
  "v_mfma_f32_16x16x32_bf16 a[144:147], v[56:59], v[56:59], a[144:147]\n" \
  "v_mfma_f32_16x16x32_bf16 a[144:147], v[60:63], v[60:63], a[144:147]\n" \
  "v_mfma_f32_16x16x32_bf16 a[148:151], v[64:67], v[64:67], 0\n" \
  "v_mfma_f32_16x16x32_bf16 a[148:151], v[68:71], v[68:71], a[148:151]\n" \
  "v_mfma_f32_16x16x32_bf16 a[148:151], v[72:75], v[72:75], a[148:151]\n" \
  "v_mfma_f32_16x16x32_bf16 a[148:151], v[76:79], v[76:79], a[148:151]\n" \
  "s_mov_b64 exec, s[66:67]\n" \
  "buffer_load_dword v138, v136, s[36:39], s51 offen offset:0 sc1\n" \
  "s_mov_b64 exec, -1\n" \
  "ds_read_b128 v[80:83], v32 offset:0\n" \
  "ds_read_b128 v[84:87], v33 offset:0\n" \
  "ds_read_b128 v[88:91], v34 offset:0\n" \
  "ds_read_b128 v[92:95], v35 offset:0\n" \
  "ds_read_b128 v[96:99], v32 offset:1024\n" \
  "ds_read_b128 v[100:103], v33 offset:1024\n" \
  "ds_read_b128 v[104:107], v34 offset:1024\n" \
  "ds_read_b128 v[108:111], v35 offset:1024\n" \
  "s_waitcnt lgkmcnt(7)\n" \
  "v_mfma_f32_16x16x32_bf16 v[112:115], v[80:83], v[48:51], 0\n" \
  "v_mfma_f32_16x16x32_bf16 v[116:119], v[80:83], v[64:67], 0\n" \
  "v_mfma_f32_16x16x32_bf16 a[152:155], v[80:83], v[80:83], 0\n" \
  "s_waitcnt lgkmcnt(6)\n" \
  "v_mfma_f32_16x16x32_bf16 v[112:115], v[84:87], v[52:55], v[112:115]\n" \
  "v_mfma_f32_16x16x32_bf16 v[116:119], v[84:87], v[68:71], v[116:119]\n" \
  "s_waitcnt lgkmcnt(5)\n" \
  "v_mfma_f32_16x16x32_bf16 v[112:115], v[88:91], v[56:59], v[112:115]\n" \
  "v_mfma_f32_16x16x32_bf16 v[116:119], v[88:91], v[72:75], v[116:119]\n" \
  "s_waitcnt lgkmcnt(4)\n" \
  "v_mfma_f32_16x16x32_bf16 v[112:115], v[92:95], v[60:63], v[112:115]\n" \
  "v_mfma_f32_16x16x32_bf16 v[116:119], v[92:95], v[76:79], v[116:119]\n" \
  "ds_read_b128 v[80:83], v32 offset:8192\n" \
  "ds_read_b128 v[84:87], v33 offset:8192\n" \
  "ds_read_b128 v[88:91], v34 offset:8192\n" \
  "ds_read_b128 v[92:95], v35 offset:8192\n" \
  "s_waitcnt lgkmcnt(7)\n" \
  "v_mfma_f32_16x16x32_bf16 v[120:123], v[96:99], v[48:51], 0\n" \
  "v_mfma_f32_16x16x32_bf16 v[124:127], v[96:99], v[64:67], 0\n" \
  "v_mfma_f32_16x16x32_bf16 a[152:155], v[96:99], v[96:99], a[152:155]\n" \
  "s_waitcnt lgkmcnt(6)\n" \
  "v_mfma_f32_16x16x32_bf16 v[120:123], v[100:103], v[52:55], v[120:123]\n" \
  "v_mfma_f32_16x16x32_bf16 v[124:127], v[100:103], v[68:71], v[124:127]\n" \
  "v_cvt_pk_bf16_f32 v128, v112, v113\n" \
  "v_cvt_pk_bf16_f32 v129, v114, v115\n" \
  "v_cvt_pk_bf16_f32 v192, v116, v117\n" \
  "v_cvt_pk_bf16_f32 v193, v118, v119\n" \
  "s_waitcnt lgkmcnt(5)\n" \
  "v_mfma_f32_16x16x32_bf16 v[120:123], v[104:107], v[56:59], v[120:123]\n" \
  "v_mfma_f32_16x16x32_bf16 v[124:127], v[104:107], v[72:75], v[124:127]\n" \
  "s_waitcnt lgkmcnt(4)\n" \
  "v_mfma_f32_16x16x32_bf16 v[120:123], v[108:111], v[60:63], v[120:123]\n" \
  "v_mfma_f32_16x16x32_bf16 v[124:127], v[108:111], v[76:79], v[124:127]\n" \
  "ds_read_b128 v[96:99], v32 offset:9216\n" \
  "ds_read_b128 v[100:103], v33 offset:9216\n" \
  "ds_read_b128 v[104:107], v34 offset:9216\n" \
  "ds_read_b128 v[108:111], v35 offset:9216\n" \
  "s_waitcnt lgkmcnt(7)\n" \
  "v_mfma_f32_16x16x32_bf16 v[112:115], v[80:83], v[48:51], 0\n" \
  "v_mfma_f32_16x16x32_bf16 v[116:119], v[80:83], v[64:67], 0\n" \
  "v_mfma_f32_16x16x32_bf16 a[152:155], v[80:83], v[80:83], a[152:155]\n" \
  "s_waitcnt lgkmcnt(6)\n" \
  "v_mfma_f32_16x16x32_bf16 v[112:115], v[84:87], v[52:55], v[112:115]\n" \
  "v_mfma_f32_16x16x32_bf16 v[116:119], v[84:87], v[68:71], v[116:119]\n" \
  "v_cvt_pk_bf16_f32 v130, v120, v121\n" \
  "v_cvt_pk_bf16_f32 v131, v122, v123\n" \
  "v_cvt_pk_bf16_f32 v194, v124, v125\n" \
  "v_cvt_pk_bf16_f32 v195, v126, v127\n" \
  "v_mfma_f32_16x16x32_bf16 a[136:139], v[128:131], v[128:131], 0\n" \
  "s_nop 0\n" \
  "v_mfma_f32_16x16x32_bf16 a[140:143], v[192:195], v[192:195], 0\n" \
  "s_waitcnt lgkmcnt(5)\n" \
  "v_mfma_f32_16x16x32_bf16 v[112:115], v[88:91], v[56:59], v[112:115]\n" \
  "v_mfma_f32_16x16x32_bf16 v[116:119], v[88:91], v[72:75], v[116:119]\n" \
  "s_waitcnt lgkmcnt(4)\n" \
  "v_mfma_f32_16x16x32_bf16 v[112:115], v[92:95], v[60:63], v[112:115]\n" \
  "v_mfma_f32_16x16x32_bf16 v[116:119], v[92:95], v[76:79], v[116:119]\n" \
  "ds_read_b128 v[80:83], v36 offset:0\n" \
  "ds_read_b128 v[84:87], v37 offset:0\n" \
  "ds_read_b128 v[88:91], v38 offset:0\n" \
  "ds_read_b128 v[92:95], v39 offset:0\n" \
  "s_waitcnt lgkmcnt(7)\n" \
  "v_mfma_f32_16x16x32_bf16 v[120:123], v[96:99], v[48:51], 0\n" \
  "v_mfma_f32_16x16x32_bf16 v[124:127], v[96:99], v[64:67], 0\n" \
  "v_mfma_f32_16x16x32_bf16 a[152:155], v[96:99], v[96:99], a[152:155]\n" \
  "s_waitcnt lgkmcnt(6)\n" \
  "v_mfma_f32_16x16x32_bf16 v[120:123], v[100:103], v[52:55], v[120:123]\n" \
  "v_mfma_f32_16x16x32_bf16 v[124:127], v[100:103], v[68:71], v[124:127]\n" \
  "v_cvt_pk_bf16_f32 v132, v112, v113\n" \
  "v_cvt_pk_bf16_f32 v133, v114, v115\n" \
  "v_cvt_pk_bf16_f32 v196, v116, v117\n" \
  "v_cvt_pk_bf16_f32 v197, v118, v119\n" \
  "s_waitcnt lgkmcnt(5)\n" \
  "v_mfma_f32_16x16x32_bf16 v[120:123], v[104:107], v[56:59], v[120:123]\n" \
  "v_mfma_f32_16x16x32_bf16 v[124:127], v[104:107], v[72:75], v[124:127]\n" \
  "s_waitcnt lgkmcnt(4)\n" \
  "v_mfma_f32_16x16x32_bf16 v[120:123], v[108:111], v[60:63], v[120:123]\n" \
  "v_mfma_f32_16x16x32_bf16 v[124:127], v[108:111], v[76:79], v[124:127]\n" \
  "ds_read_b128 v[96:99], v36 offset:1024\n" \
  "ds_read_b128 v[100:103], v37 offset:1024\n" \
  "ds_read_b128 v[104:107], v38 offset:1024\n" \
  "ds_read_b128 v[108:111], v39 offset:1024\n" \
  "s_waitcnt lgkmcnt(7)\n" \
  "v_mfma_f32_16x16x32_bf16 v[112:115], v[80:83], v[48:51], 0\n" \
  "v_mfma_f32_16x16x32_bf16 v[116:119], v[80:83], v[64:67], 0\n" \
  "v_mfma_f32_16x16x32_bf16 a[152:155], v[80:83], v[80:83], a[152:155]\n" \
  "s_waitcnt lgkmcnt(6)\n" \
  "v_mfma_f32_16x16x32_bf16 v[112:115], v[84:87], v[52:55], v[112:115]\n" \
  "v_mfma_f32_16x16x32_bf16 v[116:119], v[84:87], v[68:71], v[116:119]\n" \
  "v_cvt_pk_bf16_f32 v134, v120, v121\n" \
  "v_cvt_pk_bf16_f32 v135, v122, v123\n" \
  "v_cvt_pk_bf16_f32 v198, v124, v125\n" \
  "v_cvt_pk_bf16_f32 v199, v126, v127\n" \
  "v_mfma_f32_16x16x32_bf16 a[136:139], v[132:135], v[132:135], a[136:139]\n" \
  "s_nop 0\n" \
  "v_mfma_f32_16x16x32_bf16 a[140:143], v[196:199], v[196:199], a[140:143]\n" \
  "s_waitcnt lgkmcnt(5)\n" \
  "v_mfma_f32_16x16x32_bf16 v[112:115], v[88:91], v[56:59], v[112:115]\n" \
  "v_mfma_f32_16x16x32_bf16 v[116:119], v[88:91], v[72:75], v[116:119]\n" \
  "s_waitcnt lgkmcnt(4)\n" \
  "v_mfma_f32_16x16x32_bf16 v[112:115], v[92:95], v[60:63], v[112:115]\n" \
  "v_mfma_f32_16x16x32_bf16 v[116:119], v[92:95], v[76:79], v[116:119]\n" \
  "ds_read_b128 v[80:83], v36 offset:8192\n" \
  "ds_read_b128 v[84:87], v37 offset:8192\n" \
  "ds_read_b128 v[88:91], v38 offset:8192\n" \
  "ds_read_b128 v[92:95], v39 offset:8192\n" \
  "s_waitcnt lgkmcnt(7)\n" \
  "v_mfma_f32_16x16x32_bf16 v[120:123], v[96:99], v[48:51], 0\n" \
  "v_mfma_f32_16x16x32_bf16 v[124:127], v[96:99], v[64:67], 0\n" \
  "v_mfma_f32_16x16x32_bf16 a[152:155], v[96:99], v[96:99], a[152:155]\n" \
  "s_waitcnt lgkmcnt(6)\n" \
  "v_mfma_f32_16x16x32_bf16 v[120:123], v[100:103], v[52:55], v[120:123]\n" \
  "v_mfma_f32_16x16x32_bf16 v[124:127], v[100:103], v[68:71], v[124:127]\n" \
  "v_cvt_pk_bf16_f32 v144, v112, v113\n" \
  "v_cvt_pk_bf16_f32 v145, v114, v115\n" \
  "v_cvt_pk_bf16_f32 v208, v116, v117\n" \
  "v_cvt_pk_bf16_f32 v209, v118, v119\n" \
  "s_waitcnt lgkmcnt(5)\n" \
  "v_mfma_f32_16x16x32_bf16 v[120:123], v[104:107], v[56:59], v[120:123]\n" \
  "v_mfma_f32_16x16x32_bf16 v[124:127], v[104:107], v[72:75], v[124:127]\n" \
  "s_waitcnt lgkmcnt(4)\n" \
  "v_mfma_f32_16x16x32_bf16 v[120:123], v[108:111], v[60:63], v[120:123]\n" \
  "v_mfma_f32_16x16x32_bf16 v[124:127], v[108:111], v[76:79], v[124:127]\n" \
  "ds_read_b128 v[96:99], v36 offset:9216\n" \
  "ds_read_b128 v[100:103], v37 offset:9216\n" \
  "ds_read_b128 v[104:107], v38 offset:9216\n" \
  "ds_read_b128 v[108:111], v39 offset:9216\n" \
  "s_waitcnt lgkmcnt(7)\n" \
  "v_mfma_f32_16x16x32_bf16 v[112:115], v[80:83], v[48:51], 0\n" \
  "v_mfma_f32_16x16x32_bf16 v[116:119], v[80:83], v[64:67], 0\n" \
  "v_mfma_f32_16x16x32_bf16 a[152:155], v[80:83], v[80:83], a[152:155]\n" \
  "s_waitcnt lgkmcnt(6)\n" \
  "v_mfma_f32_16x16x32_bf16 v[112:115], v[84:87], v[52:55], v[112:115]\n" \
  "v_mfma_f32_16x16x32_bf16 v[116:119], v[84:87], v[68:71], v[116:119]\n" \
  "v_cvt_pk_bf16_f32 v146, v120, v121\n" \
  "v_cvt_pk_bf16_f32 v147, v122, v123\n" \
  "v_cvt_pk_bf16_f32 v210, v124, v125\n" \
  "v_cvt_pk_bf16_f32 v211, v126, v127\n" \
  "v_mfma_f32_16x16x32_bf16 a[136:139], v[144:147], v[144:147], a[136:139]\n" \
  "s_nop 0\n" \
  "v_mfma_f32_16x16x32_bf16 a[140:143], v[208:211], v[208:211], a[140:143]\n" \
  "s_waitcnt lgkmcnt(5)\n" \
  "v_mfma_f32_16x16x32_bf16 v[112:115], v[88:91], v[56:59], v[112:115]\n" \
  "v_mfma_f32_16x16x32_bf16 v[116:119], v[88:91], v[72:75], v[116:119]\n" \
  "s_waitcnt lgkmcnt(4)\n" \
  "v_mfma_f32_16x16x32_bf16 v[112:115], v[92:95], v[60:63], v[112:115]\n" \
  "v_mfma_f32_16x16x32_bf16 v[116:119], v[92:95], v[76:79], v[116:119]\n" \
  "ds_read_b128 v[80:83], v40 offset:0\n" \
  "ds_read_b128 v[84:87], v41 offset:0\n" \
  "ds_read_b128 v[88:91], v42 offset:0\n" \
  "ds_read_b128 v[92:95], v43 offset:0\n" \
  "s_waitcnt lgkmcnt(7)\n" \
  "v_mfma_f32_16x16x32_bf16 v[120:123], v[96:99], v[48:51], 0\n" \
  "v_mfma_f32_16x16x32_bf16 v[124:127], v[96:99], v[64:67], 0\n" \
  "v_mfma_f32_16x16x32_bf16 a[152:155], v[96:99], v[96:99], a[152:155]\n" \
  "s_waitcnt lgkmcnt(6)\n" \
  "v_mfma_f32_16x16x32_bf16 v[120:123], v[100:103], v[52:55], v[120:123]\n" \
  "v_mfma_f32_16x16x32_bf16 v[124:127], v[100:103], v[68:71], v[124:127]\n" \
  "v_cvt_pk_bf16_f32 v148, v112, v113\n" \
  "v_cvt_pk_bf16_f32 v149, v114, v115\n" \
  "v_cvt_pk_bf16_f32 v212, v116, v117\n" \
  "v_cvt_pk_bf16_f32 v213, v118, v119\n" \
  "s_waitcnt lgkmcnt(5)\n" \
  "v_mfma_f32_16x16x32_bf16 v[120:123], v[104:107], v[56:59], v[120:123]\n" \
  "v_mfma_f32_16x16x32_bf16 v[124:127], v[104:107], v[72:75], v[124:127]\n" \
  "s_waitcnt lgkmcnt(4)\n" \
  "v_mfma_f32_16x16x32_bf16 v[120:123], v[108:111], v[60:63], v[120:123]\n" \
  "v_mfma_f32_16x16x32_bf16 v[124:127], v[108:111], v[76:79], v[124:127]\n" \
  "ds_read_b128 v[96:99], v40 offset:1024\n" \
  "ds_read_b128 v[100:103], v41 offset:1024\n" \
  "ds_read_b128 v[104:107], v42 offset:1024\n" \
  "ds_read_b128 v[108:111], v43 offset:1024\n" \
  "s_waitcnt lgkmcnt(7)\n" \
  "v_mfma_f32_16x16x32_bf16 v[112:115], v[80:83], v[48:51], 0\n" \
  "v_mfma_f32_16x16x32_bf16 v[116:119], v[80:83], v[64:67], 0\n" \
  "v_mfma_f32_16x16x32_bf16 a[152:155], v[80:83], v[80:83], a[152:155]\n" \
  "s_waitcnt lgkmcnt(6)\n" \
  "v_mfma_f32_16x16x32_bf16 v[112:115], v[84:87], v[52:55], v[112:115]\n" \
  "v_mfma_f32_16x16x32_bf16 v[116:119], v[84:87], v[68:71], v[116:119]\n" \
  "v_cvt_pk_bf16_f32 v150, v120, v121\n" \
  "v_cvt_pk_bf16_f32 v151, v122, v123\n" \
  "v_cvt_pk_bf16_f32 v214, v124, v125\n" \
  "v_cvt_pk_bf16_f32 v215, v126, v127\n" \
  "v_mfma_f32_16x16x32_bf16 a[136:139], v[148:151], v[148:151], a[136:139]\n" \
  "s_nop 0\n" \
  "v_mfma_f32_16x16x32_bf16 a[140:143], v[212:215], v[212:215], a[140:143]\n" \
  "s_waitcnt lgkmcnt(5)\n" \
  "v_mfma_f32_16x16x32_bf16 v[112:115], v[88:91], v[56:59], v[112:115]\n" \
  "v_mfma_f32_16x16x32_bf16 v[116:119], v[88:91], v[72:75], v[116:119]\n" \
  "s_waitcnt lgkmcnt(4)\n" \
  "v_mfma_f32_16x16x32_bf16 v[112:115], v[92:95], v[60:63], v[112:115]\n" \
  "v_mfma_f32_16x16x32_bf16 v[116:119], v[92:95], v[76:79], v[116:119]\n" \
  "ds_read_b128 v[80:83], v40 offset:8192\n" \
  "ds_read_b128 v[84:87], v41 offset:8192\n" \
  "ds_read_b128 v[88:91], v42 offset:8192\n" \
  "ds_read_b128 v[92:95], v43 offset:8192\n" \
  "s_waitcnt lgkmcnt(7)\n" \
  "v_mfma_f32_16x16x32_bf16 v[120:123], v[96:99], v[48:51], 0\n" \
  "v_mfma_f32_16x16x32_bf16 v[124:127], v[96:99], v[64:67], 0\n" \
  "v_mfma_f32_16x16x32_bf16 a[152:155], v[96:99], v[96:99], a[152:155]\n" \
  "s_waitcnt lgkmcnt(6)\n" \
  "v_mfma_f32_16x16x32_bf16 v[120:123], v[100:103], v[52:55], v[120:123]\n" \
  "v_mfma_f32_16x16x32_bf16 v[124:127], v[100:103], v[68:71], v[124:127]\n" \
  "v_cvt_pk_bf16_f32 v160, v112, v113\n" \
  "v_cvt_pk_bf16_f32 v161, v114, v115\n" \
  "v_cvt_pk_bf16_f32 v224, v116, v117\n" \
  "v_cvt_pk_bf16_f32 v225, v118, v119\n" \
  "s_waitcnt lgkmcnt(5)\n" \
  "v_mfma_f32_16x16x32_bf16 v[120:123], v[104:107], v[56:59], v[120:123]\n" \
  "v_mfma_f32_16x16x32_bf16 v[124:127], v[104:107], v[72:75], v[124:127]\n" \
  "s_waitcnt lgkmcnt(4)\n" \
  "v_mfma_f32_16x16x32_bf16 v[120:123], v[108:111], v[60:63], v[120:123]\n" \
  "v_mfma_f32_16x16x32_bf16 v[124:127], v[108:111], v[76:79], v[124:127]\n" \
  "ds_read_b128 v[96:99], v40 offset:9216\n" \
  "ds_read_b128 v[100:103], v41 offset:9216\n" \
  "ds_read_b128 v[104:107], v42 offset:9216\n" \
  "ds_read_b128 v[108:111], v43 offset:9216\n" \
  "s_waitcnt lgkmcnt(7)\n" \
  "v_mfma_f32_16x16x32_bf16 v[112:115], v[80:83], v[48:51], 0\n" \
  "v_mfma_f32_16x16x32_bf16 v[116:119], v[80:83], v[64:67], 0\n" \
  "v_mfma_f32_16x16x32_bf16 a[152:155], v[80:83], v[80:83], a[152:155]\n" \
  "s_waitcnt lgkmcnt(6)\n" \
  "v_mfma_f32_16x16x32_bf16 v[112:115], v[84:87], v[52:55], v[112:115]\n" \
  "v_mfma_f32_16x16x32_bf16 v[116:119], v[84:87], v[68:71], v[116:119]\n" \
  "v_cvt_pk_bf16_f32 v162, v120, v121\n" \
  "v_cvt_pk_bf16_f32 v163, v122, v123\n" \
  "v_cvt_pk_bf16_f32 v226, v124, v125\n" \
  "v_cvt_pk_bf16_f32 v227, v126, v127\n" \
  "v_mfma_f32_16x16x32_bf16 a[136:139], v[160:163], v[160:163], a[136:139]\n" \
  "s_nop 0\n" \
  "v_mfma_f32_16x16x32_bf16 a[140:143], v[224:227], v[224:227], a[140:143]\n" \
  "s_waitcnt lgkmcnt(5)\n" \
  "v_mfma_f32_16x16x32_bf16 v[112:115], v[88:91], v[56:59], v[112:115]\n" \
  "v_mfma_f32_16x16x32_bf16 v[116:119], v[88:91], v[72:75], v[116:119]\n" \
  "s_waitcnt lgkmcnt(4)\n" \
  "v_mfma_f32_16x16x32_bf16 v[112:115], v[92:95], v[60:63], v[112:115]\n" \
  "v_mfma_f32_16x16x32_bf16 v[116:119], v[92:95], v[76:79], v[116:119]\n" \
  "ds_read_b128 v[80:83], v44 offset:0\n" \
  "ds_read_b128 v[84:87], v45 offset:0\n" \
  "ds_read_b128 v[88:91], v46 offset:0\n" \
  "ds_read_b128 v[92:95], v47 offset:0\n" \
  "s_waitcnt lgkmcnt(7)\n" \
  "v_mfma_f32_16x16x32_bf16 v[120:123], v[96:99], v[48:51], 0\n" \
  "v_mfma_f32_16x16x32_bf16 v[124:127], v[96:99], v[64:67], 0\n" \
  "v_mfma_f32_16x16x32_bf16 a[152:155], v[96:99], v[96:99], a[152:155]\n" \
  "s_waitcnt lgkmcnt(6)\n" \
  "v_mfma_f32_16x16x32_bf16 v[120:123], v[100:103], v[52:55], v[120:123]\n" \
  "v_mfma_f32_16x16x32_bf16 v[124:127], v[100:103], v[68:71], v[124:127]\n" \
  "v_cvt_pk_bf16_f32 v164, v112, v113\n" \
  "v_cvt_pk_bf16_f32 v165, v114, v115\n" \
  "v_cvt_pk_bf16_f32 v228, v116, v117\n" \
  "v_cvt_pk_bf16_f32 v229, v118, v119\n" \
  "s_waitcnt lgkmcnt(5)\n" \
  "v_mfma_f32_16x16x32_bf16 v[120:123], v[104:107], v[56:59], v[120:123]\n" \
  "v_mfma_f32_16x16x32_bf16 v[124:127], v[104:107], v[72:75], v[124:127]\n" \
  "s_waitcnt lgkmcnt(4)\n" \
  "v_mfma_f32_16x16x32_bf16 v[120:123], v[108:111], v[60:63], v[120:123]\n" \
  "v_mfma_f32_16x16x32_bf16 v[124:127], v[108:111], v[76:79], v[124:127]\n" \
  "ds_read_b128 v[96:99], v44 offset:1024\n" \
  "ds_read_b128 v[100:103], v45 offset:1024\n" \
  "ds_read_b128 v[104:107], v46 offset:1024\n" \
  "ds_read_b128 v[108:111], v47 offset:1024\n" \
  "s_waitcnt lgkmcnt(7)\n" \
  "v_mfma_f32_16x16x32_bf16 v[112:115], v[80:83], v[48:51], 0\n" \
  "v_mfma_f32_16x16x32_bf16 v[116:119], v[80:83], v[64:67], 0\n" \
  "v_mfma_f32_16x16x32_bf16 a[152:155], v[80:83], v[80:83], a[152:155]\n" \
  "s_waitcnt lgkmcnt(6)\n" \
  "v_mfma_f32_16x16x32_bf16 v[112:115], v[84:87], v[52:55], v[112:115]\n" \
  "v_mfma_f32_16x16x32_bf16 v[116:119], v[84:87], v[68:71], v[116:119]\n" \
  "v_cvt_pk_bf16_f32 v166, v120, v121\n" \
  "v_cvt_pk_bf16_f32 v167, v122, v123\n" \
  "v_cvt_pk_bf16_f32 v230, v124, v125\n" \
  "v_cvt_pk_bf16_f32 v231, v126, v127\n" \
  "v_mfma_f32_16x16x32_bf16 a[136:139], v[164:167], v[164:167], a[136:139]\n" \
  "s_nop 0\n" \
  "v_mfma_f32_16x16x32_bf16 a[140:143], v[228:231], v[228:231], a[140:143]\n" \
  "s_waitcnt lgkmcnt(5)\n" \
  "v_mfma_f32_16x16x32_bf16 v[112:115], v[88:91], v[56:59], v[112:115]\n" \
  "v_mfma_f32_16x16x32_bf16 v[116:119], v[88:91], v[72:75], v[116:119]\n" \
  "s_waitcnt lgkmcnt(4)\n" \
  "v_mfma_f32_16x16x32_bf16 v[112:115], v[92:95], v[60:63], v[112:115]\n" \
  "v_mfma_f32_16x16x32_bf16 v[116:119], v[92:95], v[76:79], v[116:119]\n" \
  "ds_read_b128 v[80:83], v44 offset:8192\n" \
  "ds_read_b128 v[84:87], v45 offset:8192\n" \
  "ds_read_b128 v[88:91], v46 offset:8192\n" \
  "ds_read_b128 v[92:95], v47 offset:8192\n" \
  "s_waitcnt lgkmcnt(7)\n" \
  "v_mfma_f32_16x16x32_bf16 v[120:123], v[96:99], v[48:51], 0\n" \
  "v_mfma_f32_16x16x32_bf16 v[124:127], v[96:99], v[64:67], 0\n" \
  "v_mfma_f32_16x16x32_bf16 a[152:155], v[96:99], v[96:99], a[152:155]\n" \
  "s_waitcnt lgkmcnt(6)\n" \
  "v_mfma_f32_16x16x32_bf16 v[120:123], v[100:103], v[52:55], v[120:123]\n" \
  "v_mfma_f32_16x16x32_bf16 v[124:127], v[100:103], v[68:71], v[124:127]\n" \
  "v_cvt_pk_bf16_f32 v176, v112, v113\n" \
  "v_cvt_pk_bf16_f32 v177, v114, v115\n" \
  "v_cvt_pk_bf16_f32 v240, v116, v117\n" \
  "v_cvt_pk_bf16_f32 v241, v118, v119\n" \
  "s_waitcnt lgkmcnt(5)\n" \
  "v_mfma_f32_16x16x32_bf16 v[120:123], v[104:107], v[56:59], v[120:123]\n" \
  "v_mfma_f32_16x16x32_bf16 v[124:127], v[104:107], v[72:75], v[124:127]\n" \
  "s_waitcnt lgkmcnt(4)\n" \
  "v_mfma_f32_16x16x32_bf16 v[120:123], v[108:111], v[60:63], v[120:123]\n" \
  "v_mfma_f32_16x16x32_bf16 v[124:127], v[108:111], v[76:79], v[124:127]\n" \
  "ds_read_b128 v[96:99], v44 offset:9216\n" \
  "ds_read_b128 v[100:103], v45 offset:9216\n" \
  "ds_read_b128 v[104:107], v46 offset:9216\n" \
  "ds_read_b128 v[108:111], v47 offset:9216\n" \
  "s_waitcnt lgkmcnt(7)\n" \
  "v_mfma_f32_16x16x32_bf16 v[112:115], v[80:83], v[48:51], 0\n" \
  "v_mfma_f32_16x16x32_bf16 v[116:119], v[80:83], v[64:67], 0\n" \
  "v_mfma_f32_16x16x32_bf16 a[152:155], v[80:83], v[80:83], a[152:155]\n" \
  "s_waitcnt lgkmcnt(6)\n" \
  "v_mfma_f32_16x16x32_bf16 v[112:115], v[84:87], v[52:55], v[112:115]\n" \
  "v_mfma_f32_16x16x32_bf16 v[116:119], v[84:87], v[68:71], v[116:119]\n" \
  "v_cvt_pk_bf16_f32 v178, v120, v121\n" \
  "v_cvt_pk_bf16_f32 v179, v122, v123\n" \
  "v_cvt_pk_bf16_f32 v242, v124, v125\n" \
  "v_cvt_pk_bf16_f32 v243, v126, v127\n" \
  "v_mfma_f32_16x16x32_bf16 a[136:139], v[176:179], v[176:179], a[136:139]\n" \
  "s_nop 0\n" \
  "v_mfma_f32_16x16x32_bf16 a[140:143], v[240:243], v[240:243], a[140:143]\n" \
  "s_waitcnt lgkmcnt(5)\n" \
  "v_mfma_f32_16x16x32_bf16 v[112:115], v[88:91], v[56:59], v[112:115]\n" \
  "v_mfma_f32_16x16x32_bf16 v[116:119], v[88:91], v[72:75], v[116:119]\n" \
  "s_waitcnt lgkmcnt(4)\n" \
  "v_mfma_f32_16x16x32_bf16 v[112:115], v[92:95], v[60:63], v[112:115]\n" \
  "v_mfma_f32_16x16x32_bf16 v[116:119], v[92:95], v[76:79], v[116:119]\n" \
  "s_waitcnt lgkmcnt(3)\n" \
  "v_mfma_f32_16x16x32_bf16 v[120:123], v[96:99], v[48:51], 0\n" \
  "v_mfma_f32_16x16x32_bf16 v[124:127], v[96:99], v[64:67], 0\n" \
  "v_mfma_f32_16x16x32_bf16 a[152:155], v[96:99], v[96:99], a[152:155]\n" \
  "s_waitcnt lgkmcnt(2)\n" \
  "v_mfma_f32_16x16x32_bf16 v[120:123], v[100:103], v[52:55], v[120:123]\n" \
  "v_mfma_f32_16x16x32_bf16 v[124:127], v[100:103], v[68:71], v[124:127]\n" \
  "v_cvt_pk_bf16_f32 v180, v112, v113\n" \
  "v_cvt_pk_bf16_f32 v181, v114, v115\n" \
  "v_cvt_pk_bf16_f32 v244, v116, v117\n" \
  "v_cvt_pk_bf16_f32 v245, v118, v119\n" \
  "s_waitcnt lgkmcnt(1)\n" \
  "v_mfma_f32_16x16x32_bf16 v[120:123], v[104:107], v[56:59], v[120:123]\n" \
  "v_mfma_f32_16x16x32_bf16 v[124:127], v[104:107], v[72:75], v[124:127]\n" \
  "s_waitcnt lgkmcnt(0)\n" \
  "v_mfma_f32_16x16x32_bf16 v[120:123], v[108:111], v[60:63], v[120:123]\n" \
  "v_mfma_f32_16x16x32_bf16 v[124:127], v[108:111], v[76:79], v[124:127]\n" \
  "s_nop 6\n" \
  "v_cvt_pk_bf16_f32 v182, v120, v121\n" \
  "v_cvt_pk_bf16_f32 v183, v122, v123\n" \
  "v_cvt_pk_bf16_f32 v246, v124, v125\n" \
  "v_cvt_pk_bf16_f32 v247, v126, v127\n" \
  "v_mfma_f32_16x16x32_bf16 a[136:139], v[180:183], v[180:183], a[136:139]\n" \
  "s_nop 0\n" \
  "v_mfma_f32_16x16x32_bf16 a[140:143], v[244:247], v[244:247], a[140:143]\n" \
  "s_waitcnt lgkmcnt(0)\n" \
  "s_barrier\n" \
  "s_mov_b64 s[40:41], %[pWq]\n" \
  "s_add_u32 m0, s45, 0\n" \
  "s_add_u32 s58, s46, 524288\n" \
  "buffer_load_dwordx4 v8, s[40:43], s58 offen lds\n" \
  "s_add_u32 m0, s45, 1024\n" \
  "s_add_u32 s58, s46, 557056\n" \
  "buffer_load_dwordx4 v8, s[40:43], s58 offen lds\n" \
  "s_add_u32 m0, s45, 2048\n" \
  "s_add_u32 s58, s46, 589824\n" \
  "buffer_load_dwordx4 v9, s[40:43], s58 offen lds\n" \
  "s_add_u32 m0, s45, 3072\n" \
  "s_add_u32 s58, s46, 622592\n" \
  "buffer_load_dwordx4 v9, s[40:43], s58 offen lds\n" \
  "s_add_u32 m0, s45, 4096\n" \
  "s_add_u32 s58, s46, 655360\n" \
  "buffer_load_dwordx4 v8, s[40:43], s58 offen lds\n" \
  "s_add_u32 m0, s45, 5120\n" \
  "s_add_u32 s58, s46, 688128\n" \
  "buffer_load_dwordx4 v8, s[40:43], s58 offen lds\n" \
  "s_add_u32 m0, s45, 6144\n" \
  "s_add_u32 s58, s46, 720896\n" \
  "buffer_load_dwordx4 v9, s[40:43], s58 offen lds\n" \
  "s_add_u32 m0, s45, 7168\n" \
  "s_add_u32 s58, s46, 753664\n" \
  "buffer_load_dwordx4 v9, s[40:43], s58 offen lds\n" \
  "s_add_u32 m0, s45, 8192\n" \
  "s_add_u32 s58, s46, 786432\n" \
  "buffer_load_dwordx4 v8, s[40:43], s58 offen lds\n" \
  "s_add_u32 m0, s45, 9216\n" \
  "s_add_u32 s58, s46, 819200\n" \
  "buffer_load_dwordx4 v8, s[40:43], s58 offen lds\n" \
  "s_add_u32 m0, s45, 10240\n" \
  "s_add_u32 s58, s46, 851968\n" \
  "buffer_load_dwordx4 v9, s[40:43], s58 offen lds\n" \
  "s_add_u32 m0, s45, 11264\n" \
  "s_add_u32 s58, s46, 884736\n" \
  "buffer_load_dwordx4 v9, s[40:43], s58 offen lds\n" \
  "s_add_u32 m0, s45, 12288\n" \
  "s_add_u32 s58, s46, 917504\n" \
  "buffer_load_dwordx4 v8, s[40:43], s58 offen lds\n" \
  "s_add_u32 m0, s45, 13312\n" \
  "s_add_u32 s58, s46, 950272\n" \
  "buffer_load_dwordx4 v8, s[40:43], s58 offen lds\n" \
  "s_add_u32 m0, s45, 14336\n" \
  "s_add_u32 s58, s46, 983040\n" \
  "buffer_load_dwordx4 v9, s[40:43], s58 offen lds\n" \
  "s_add_u32 m0, s45, 15360\n" \
  "s_add_u32 s58, s46, 1015808\n" \
  "buffer_load_dwordx4 v9, s[40:43], s58 offen lds\n" \
  "v_add_u32 v32, 65536, v32\n" \
  "v_add_u32 v33, 65536, v33\n" \
  "v_add_u32 v34, 65536, v34\n" \
  "v_add_u32 v35, 65536, v35\n" \
  "v_add_u32 v36, 65536, v36\n" \
  "v_add_u32 v37, 65536, v37\n" \
  "v_add_u32 v38, 65536, v38\n" \
  "v_add_u32 v39, 65536, v39\n" \
  "v_add_u32 v40, 65536, v40\n" \
  "v_add_u32 v41, 65536, v41\n" \
  "v_add_u32 v42, 65536, v42\n" \
  "v_add_u32 v43, 65536, v43\n" \
  "v_add_u32 v44, 65536, v44\n" \
  "v_add_u32 v45, 65536, v45\n" \
  "v_add_u32 v46, 65536, v46\n" \
  "v_add_u32 v47, 65536, v47\n" \
  "s_waitcnt vmcnt(16)\n" \
  "s_cmp_eq_u32 s76, 0\n" \
  "s_cbranch_scc0 L_hsd_%=\n" \
  "s_mov_b32 s56, 0\n" \
  "s_mov_b64 exec, s[66:67]\n" \
  "L_hs_%=:\n" \
  "v_cmp_eq_u32 vcc, v138, v137\n" \
  "s_add_u32 s56, s56, 1\n" \
  "s_nop 3\n" \
  "s_cbranch_vccz L_hs2_%=\n" \
  "s_cmp_lt_u32 s56, 400000\n" \
  "s_cbranch_scc0 L_hs2_%=\n" \
  "buffer_load_dword v138, v136, s[36:39], s51 offen sc1\n" \
  "s_waitcnt vmcnt(0)\n" \
  "s_branch L_hs_%=\n" \
  "L_hs2_%=:\n" \
  "s_add_u32 s64, s36, 4194304\n" \
  "s_addc_u32 s65, s37, 0\n" \
  "global_atomic_add v140, v139, s[64:65] offset:48\n" \
  "s_mov_b64 exec, -1\n" \
  "L_hsd_%=:\n" \
  "s_waitcnt lgkmcnt(0)\n" \
  "s_barrier\n" \
  "ds_read_b128 v[80:83], v32 offset:0\n" \
  "ds_read_b128 v[84:87], v33 offset:0\n" \
  "ds_read_b128 v[88:91], v34 offset:0\n" \
  "ds_read_b128 v[92:95], v35 offset:0\n" \
  "ds_read_b128 v[96:99], v32 offset:1024\n" \
  "ds_read_b128 v[100:103], v33 offset:1024\n" \
  "ds_read_b128 v[104:107], v34 offset:1024\n" \
  "ds_read_b128 v[108:111], v35 offset:1024\n" \
  "s_waitcnt lgkmcnt(7)\n" \
  "v_mfma_f32_16x16x32_bf16 v[112:115], v[80:83], v[48:51], 0\n" \
  "v_mfma_f32_16x16x32_bf16 v[116:119], v[80:83], v[64:67], 0\n" \
  "v_mfma_f32_16x16x32_bf16 a[152:155], v[80:83], v[80:83], a[152:155]\n" \
  "s_waitcnt lgkmcnt(6)\n" \
  "v_mfma_f32_16x16x32_bf16 v[112:115], v[84:87], v[52:55], v[112:115]\n" \
  "v_mfma_f32_16x16x32_bf16 v[116:119], v[84:87], v[68:71], v[116:119]\n" \
  "s_waitcnt lgkmcnt(5)\n" \
  "v_mfma_f32_16x16x32_bf16 v[112:115], v[88:91], v[56:59], v[112:115]\n" \
  "v_mfma_f32_16x16x32_bf16 v[116:119], v[88:91], v[72:75], v[116:119]\n" \
  "s_waitcnt lgkmcnt(4)\n" \
  "v_mfma_f32_16x16x32_bf16 v[112:115], v[92:95], v[60:63], v[112:115]\n" \
  "v_mfma_f32_16x16x32_bf16 v[116:119], v[92:95], v[76:79], v[116:119]\n" \
  "ds_read_b128 v[80:83], v32 offset:8192\n" \
  "ds_read_b128 v[84:87], v33 offset:8192\n" \
  "ds_read_b128 v[88:91], v34 offset:8192\n" \
  "ds_read_b128 v[92:95], v35 offset:8192\n" \
  "s_waitcnt lgkmcnt(7)\n" \
  "v_mfma_f32_16x16x32_bf16 v[120:123], v[96:99], v[48:51], 0\n" \
  "v_mfma_f32_16x16x32_bf16 v[124:127], v[96:99], v[64:67], 0\n" \
  "v_mfma_f32_16x16x32_bf16 a[152:155], v[96:99], v[96:99], a[152:155]\n" \
  "s_waitcnt lgkmcnt(6)\n" \
  "v_mfma_f32_16x16x32_bf16 v[120:123], v[100:103], v[52:55], v[120:123]\n" \
  "v_mfma_f32_16x16x32_bf16 v[124:127], v[100:103], v[68:71], v[124:127]\n" \
  "v_cvt_pk_bf16_f32 v136, v112, v113\n" \
  "v_cvt_pk_bf16_f32 v137, v114, v115\n" \
  "v_cvt_pk_bf16_f32 v200, v116, v117\n" \
  "v_cvt_pk_bf16_f32 v201, v118, v119\n" \
  "s_waitcnt lgkmcnt(5)\n" \
  "v_mfma_f32_16x16x32_bf16 v[120:123], v[104:107], v[56:59], v[120:123]\n" \
  "v_mfma_f32_16x16x32_bf16 v[124:127], v[104:107], v[72:75], v[124:127]\n" \
  "s_waitcnt lgkmcnt(4)\n" \
  "v_mfma_f32_16x16x32_bf16 v[120:123], v[108:111], v[60:63], v[120:123]\n" \
  "v_mfma_f32_16x16x32_bf16 v[124:127], v[108:111], v[76:79], v[124:127]\n" \
  "ds_read_b128 v[96:99], v32 offset:9216\n" \
  "ds_read_b128 v[100:103], v33 offset:9216\n" \
  "ds_read_b128 v[104:107], v34 offset:9216\n" \
  "ds_read_b128 v[108:111], v35 offset:9216\n" \
  "s_waitcnt lgkmcnt(7)\n" \
  "v_mfma_f32_16x16x32_bf16 v[112:115], v[80:83], v[48:51], 0\n" \
  "v_mfma_f32_16x16x32_bf16 v[116:119], v[80:83], v[64:67], 0\n" \
  "v_mfma_f32_16x16x32_bf16 a[152:155], v[80:83], v[80:83], a[152:155]\n" \
  "s_waitcnt lgkmcnt(6)\n" \
  "v_mfma_f32_16x16x32_bf16 v[112:115], v[84:87], v[52:55], v[112:115]\n" \
  "v_mfma_f32_16x16x32_bf16 v[116:119], v[84:87], v[68:71], v[116:119]\n" \
  "v_cvt_pk_bf16_f32 v138, v120, v121\n" \
  "v_cvt_pk_bf16_f32 v139, v122, v123\n" \
  "v_cvt_pk_bf16_f32 v202, v124, v125\n" \
  "v_cvt_pk_bf16_f32 v203, v126, v127\n" \
  "v_mfma_f32_16x16x32_bf16 a[136:139], v[136:139], v[136:139], a[136:139]\n" \
  "s_nop 0\n" \
  "v_mfma_f32_16x16x32_bf16 a[140:143], v[200:203], v[200:203], a[140:143]\n" \
  "s_waitcnt lgkmcnt(5)\n" \
  "v_mfma_f32_16x16x32_bf16 v[112:115], v[88:91], v[56:59], v[112:115]\n" \
  "v_mfma_f32_16x16x32_bf16 v[116:119], v[88:91], v[72:75], v[116:119]\n" \
  "s_waitcnt lgkmcnt(4)\n" \
  "v_mfma_f32_16x16x32_bf16 v[112:115], v[92:95], v[60:63], v[112:115]\n" \
  "v_mfma_f32_16x16x32_bf16 v[116:119], v[92:95], v[76:79], v[116:119]\n" \
  "ds_read_b128 v[80:83], v36 offset:0\n" \
  "ds_read_b128 v[84:87], v37 offset:0\n" \
  "ds_read_b128 v[88:91], v38 offset:0\n" \
  "ds_read_b128 v[92:95], v39 offset:0\n" \
  "s_waitcnt lgkmcnt(7)\n" \
  "v_mfma_f32_16x16x32_bf16 v[120:123], v[96:99], v[48:51], 0\n" \
  "v_mfma_f32_16x16x32_bf16 v[124:127], v[96:99], v[64:67], 0\n" \
  "v_mfma_f32_16x16x32_bf16 a[152:155], v[96:99], v[96:99], a[152:155]\n" \
  "s_waitcnt lgkmcnt(6)\n" \
  "v_mfma_f32_16x16x32_bf16 v[120:123], v[100:103], v[52:55], v[120:123]\n" \
  "v_mfma_f32_16x16x32_bf16 v[124:127], v[100:103], v[68:71], v[124:127]\n" \
  "v_cvt_pk_bf16_f32 v140, v112, v113\n" \
  "v_cvt_pk_bf16_f32 v141, v114, v115\n" \
  "v_cvt_pk_bf16_f32 v204, v116, v117\n" \
  "v_cvt_pk_bf16_f32 v205, v118, v119\n" \
  "s_waitcnt lgkmcnt(5)\n" \
  "v_mfma_f32_16x16x32_bf16 v[120:123], v[104:107], v[56:59], v[120:123]\n" \
  "v_mfma_f32_16x16x32_bf16 v[124:127], v[104:107], v[72:75], v[124:127]\n" \
  "s_waitcnt lgkmcnt(4)\n" \
  "v_mfma_f32_16x16x32_bf16 v[120:123], v[108:111], v[60:63], v[120:123]\n" \
  "v_mfma_f32_16x16x32_bf16 v[124:127], v[108:111], v[76:79], v[124:127]\n" \
  "ds_read_b128 v[96:99], v36 offset:1024\n" \
  "ds_read_b128 v[100:103], v37 offset:1024\n" \
  "ds_read_b128 v[104:107], v38 offset:1024\n" \
  "ds_read_b128 v[108:111], v39 offset:1024\n" \
  "s_waitcnt lgkmcnt(7)\n" \
  "v_mfma_f32_16x16x32_bf16 v[112:115], v[80:83], v[48:51], 0\n" \
  "v_mfma_f32_16x16x32_bf16 v[116:119], v[80:83], v[64:67], 0\n" \
  "v_mfma_f32_16x16x32_bf16 a[152:155], v[80:83], v[80:83], a[152:155]\n" \
  "s_waitcnt lgkmcnt(6)\n" \
  "v_mfma_f32_16x16x32_bf16 v[112:115], v[84:87], v[52:55], v[112:115]\n" \
  "v_mfma_f32_16x16x32_bf16 v[116:119], v[84:87], v[68:71], v[116:119]\n" \
  "v_cvt_pk_bf16_f32 v142, v120, v121\n" \
  "v_cvt_pk_bf16_f32 v143, v122, v123\n" \
  "v_cvt_pk_bf16_f32 v206, v124, v125\n" \
  "v_cvt_pk_bf16_f32 v207, v126, v127\n" \
  "v_mfma_f32_16x16x32_bf16 a[136:139], v[140:143], v[140:143], a[136:139]\n" \
  "s_nop 0\n" \
  "v_mfma_f32_16x16x32_bf16 a[140:143], v[204:207], v[204:207], a[140:143]\n" \
  "s_waitcnt lgkmcnt(5)\n" \
  "v_mfma_f32_16x16x32_bf16 v[112:115], v[88:91], v[56:59], v[112:115]\n" \
  "v_mfma_f32_16x16x32_bf16 v[116:119], v[88:91], v[72:75], v[116:119]\n" \
  "s_waitcnt lgkmcnt(4)\n" \
  "v_mfma_f32_16x16x32_bf16 v[112:115], v[92:95], v[60:63], v[112:115]\n" \
  "v_mfma_f32_16x16x32_bf16 v[116:119], v[92:95], v[76:79], v[116:119]\n" \
  "ds_read_b128 v[80:83], v36 offset:8192\n" \
  "ds_read_b128 v[84:87], v37 offset:8192\n" \
  "ds_read_b128 v[88:91], v38 offset:8192\n" \
  "ds_read_b128 v[92:95], v39 offset:8192\n" \
  "s_waitcnt lgkmcnt(7)\n" \
  "v_mfma_f32_16x16x32_bf16 v[120:123], v[96:99], v[48:51], 0\n" \
  "v_mfma_f32_16x16x32_bf16 v[124:127], v[96:99], v[64:67], 0\n" \
  "v_mfma_f32_16x16x32_bf16 a[152:155], v[96:99], v[96:99], a[152:155]\n" \
  "s_waitcnt lgkmcnt(6)\n" \
  "v_mfma_f32_16x16x32_bf16 v[120:123], v[100:103], v[52:55], v[120:123]\n" \
  "v_mfma_f32_16x16x32_bf16 v[124:127], v[100:103], v[68:71], v[124:127]\n" \
  "v_cvt_pk_bf16_f32 v152, v112, v113\n" \
  "v_cvt_pk_bf16_f32 v153, v114, v115\n" \
  "v_cvt_pk_bf16_f32 v216, v116, v117\n" \
  "v_cvt_pk_bf16_f32 v217, v118, v119\n" \
  "s_waitcnt lgkmcnt(5)\n" \
  "v_mfma_f32_16x16x32_bf16 v[120:123], v[104:107], v[56:59], v[120:123]\n" \
  "v_mfma_f32_16x16x32_bf16 v[124:127], v[104:107], v[72:75], v[124:127]\n" \
  "s_waitcnt lgkmcnt(4)\n" \
  "v_mfma_f32_16x16x32_bf16 v[120:123], v[108:111], v[60:63], v[120:123]\n" \
  "v_mfma_f32_16x16x32_bf16 v[124:127], v[108:111], v[76:79], v[124:127]\n" \
  "ds_read_b128 v[96:99], v36 offset:9216\n" \
  "ds_read_b128 v[100:103], v37 offset:9216\n" \
  "ds_read_b128 v[104:107], v38 offset:9216\n" \
  "ds_read_b128 v[108:111], v39 offset:9216\n" \
  "s_waitcnt lgkmcnt(7)\n" \
  "v_mfma_f32_16x16x32_bf16 v[112:115], v[80:83], v[48:51], 0\n" \
  "v_mfma_f32_16x16x32_bf16 v[116:119], v[80:83], v[64:67], 0\n" \
  "v_mfma_f32_16x16x32_bf16 a[152:155], v[80:83], v[80:83], a[152:155]\n" \
  "s_waitcnt lgkmcnt(6)\n" \
  "v_mfma_f32_16x16x32_bf16 v[112:115], v[84:87], v[52:55], v[112:115]\n" \
  "v_mfma_f32_16x16x32_bf16 v[116:119], v[84:87], v[68:71], v[116:119]\n" \
  "v_cvt_pk_bf16_f32 v154, v120, v121\n" \
  "v_cvt_pk_bf16_f32 v155, v122, v123\n" \
  "v_cvt_pk_bf16_f32 v218, v124, v125\n" \
  "v_cvt_pk_bf16_f32 v219, v126, v127\n" \
  "v_mfma_f32_16x16x32_bf16 a[136:139], v[152:155], v[152:155], a[136:139]\n" \
  "s_nop 0\n" \
  "v_mfma_f32_16x16x32_bf16 a[140:143], v[216:219], v[216:219], a[140:143]\n" \
  "s_waitcnt lgkmcnt(5)\n" \
  "v_mfma_f32_16x16x32_bf16 v[112:115], v[88:91], v[56:59], v[112:115]\n" \
  "v_mfma_f32_16x16x32_bf16 v[116:119], v[88:91], v[72:75], v[116:119]\n" \
  "s_waitcnt lgkmcnt(4)\n" \
  "v_mfma_f32_16x16x32_bf16 v[112:115], v[92:95], v[60:63], v[112:115]\n" \
  "v_mfma_f32_16x16x32_bf16 v[116:119], v[92:95], v[76:79], v[116:119]\n" \
  "ds_read_b128 v[80:83], v40 offset:0\n" \
  "ds_read_b128 v[84:87], v41 offset:0\n" \
  "ds_read_b128 v[88:91], v42 offset:0\n" \
  "ds_read_b128 v[92:95], v43 offset:0\n" \
  "s_waitcnt lgkmcnt(7)\n" \
  "v_mfma_f32_16x16x32_bf16 v[120:123], v[96:99], v[48:51], 0\n" \
  "v_mfma_f32_16x16x32_bf16 v[124:127], v[96:99], v[64:67], 0\n" \
  "v_mfma_f32_16x16x32_bf16 a[152:155], v[96:99], v[96:99], a[152:155]\n" \
  "s_waitcnt lgkmcnt(6)\n" \
  "v_mfma_f32_16x16x32_bf16 v[120:123], v[100:103], v[52:55], v[120:123]\n" \
  "v_mfma_f32_16x16x32_bf16 v[124:127], v[100:103], v[68:71], v[124:127]\n" \
  "v_cvt_pk_bf16_f32 v156, v112, v113\n" \
  "v_cvt_pk_bf16_f32 v157, v114, v115\n" \
  "v_cvt_pk_bf16_f32 v220, v116, v117\n" \
  "v_cvt_pk_bf16_f32 v221, v118, v119\n" \
  "s_waitcnt lgkmcnt(5)\n" \
  "v_mfma_f32_16x16x32_bf16 v[120:123], v[104:107], v[56:59], v[120:123]\n" \
  "v_mfma_f32_16x16x32_bf16 v[124:127], v[104:107], v[72:75], v[124:127]\n" \
  "s_waitcnt lgkmcnt(4)\n" \
  "v_mfma_f32_16x16x32_bf16 v[120:123], v[108:111], v[60:63], v[120:123]\n" \
  "v_mfma_f32_16x16x32_bf16 v[124:127], v[108:111], v[76:79], v[124:127]\n" \
  "ds_read_b128 v[96:99], v40 offset:1024\n" \
  "ds_read_b128 v[100:103], v41 offset:1024\n" \
  "ds_read_b128 v[104:107], v42 offset:1024\n" \
  "ds_read_b128 v[108:111], v43 offset:1024\n" \
  "s_waitcnt lgkmcnt(7)\n" \
  "v_mfma_f32_16x16x32_bf16 v[112:115], v[80:83], v[48:51], 0\n" \
  "v_mfma_f32_16x16x32_bf16 v[116:119], v[80:83], v[64:67], 0\n" \
  "v_mfma_f32_16x16x32_bf16 a[152:155], v[80:83], v[80:83], a[152:155]\n" \
  "s_waitcnt lgkmcnt(6)\n" \
  "v_mfma_f32_16x16x32_bf16 v[112:115], v[84:87], v[52:55], v[112:115]\n" \
  "v_mfma_f32_16x16x32_bf16 v[116:119], v[84:87], v[68:71], v[116:119]\n" \
  "v_cvt_pk_bf16_f32 v158, v120, v121\n" \
  "v_cvt_pk_bf16_f32 v159, v122, v123\n" \
  "v_cvt_pk_bf16_f32 v222, v124, v125\n" \
  "v_cvt_pk_bf16_f32 v223, v126, v127\n" \
  "v_mfma_f32_16x16x32_bf16 a[136:139], v[156:159], v[156:159], a[136:139]\n" \
  "s_nop 0\n" \
  "v_mfma_f32_16x16x32_bf16 a[140:143], v[220:223], v[220:223], a[140:143]\n" \
  "s_waitcnt lgkmcnt(5)\n" \
  "v_mfma_f32_16x16x32_bf16 v[112:115], v[88:91], v[56:59], v[112:115]\n" \
  "v_mfma_f32_16x16x32_bf16 v[116:119], v[88:91], v[72:75], v[116:119]\n" \
  "s_waitcnt lgkmcnt(4)\n" \
  "v_mfma_f32_16x16x32_bf16 v[112:115], v[92:95], v[60:63], v[112:115]\n" \
  "v_mfma_f32_16x16x32_bf16 v[116:119], v[92:95], v[76:79], v[116:119]\n" \
  "ds_read_b128 v[80:83], v40 offset:8192\n" \
  "ds_read_b128 v[84:87], v41 offset:8192\n" \
  "ds_read_b128 v[88:91], v42 offset:8192\n" \
  "ds_read_b128 v[92:95], v43 offset:8192\n" \
  "s_waitcnt lgkmcnt(7)\n" \
  "v_mfma_f32_16x16x32_bf16 v[120:123], v[96:99], v[48:51], 0\n" \
  "v_mfma_f32_16x16x32_bf16 v[124:127], v[96:99], v[64:67], 0\n" \
  "v_mfma_f32_16x16x32_bf16 a[152:155], v[96:99], v[96:99], a[152:155]\n" \
  "s_waitcnt lgkmcnt(6)\n" \
  "v_mfma_f32_16x16x32_bf16 v[120:123], v[100:103], v[52:55], v[120:123]\n" \
  "v_mfma_f32_16x16x32_bf16 v[124:127], v[100:103], v[68:71], v[124:127]\n" \
  "v_cvt_pk_bf16_f32 v168, v112, v113\n" \
  "v_cvt_pk_bf16_f32 v169, v114, v115\n" \
  "v_cvt_pk_bf16_f32 v232, v116, v117\n" \
  "v_cvt_pk_bf16_f32 v233, v118, v119\n" \
  "s_waitcnt lgkmcnt(5)\n" \
  "v_mfma_f32_16x16x32_bf16 v[120:123], v[104:107], v[56:59], v[120:123]\n" \
  "v_mfma_f32_16x16x32_bf16 v[124:127], v[104:107], v[72:75], v[124:127]\n" \
  "s_waitcnt lgkmcnt(4)\n" \
  "v_mfma_f32_16x16x32_bf16 v[120:123], v[108:111], v[60:63], v[120:123]\n" \
  "v_mfma_f32_16x16x32_bf16 v[124:127], v[108:111], v[76:79], v[124:127]\n" \
  "ds_read_b128 v[96:99], v40 offset:9216\n" \
  "ds_read_b128 v[100:103], v41 offset:9216\n" \
  "ds_read_b128 v[104:107], v42 offset:9216\n" \
  "ds_read_b128 v[108:111], v43 offset:9216\n" \
  "s_waitcnt lgkmcnt(7)\n" \
  "v_mfma_f32_16x16x32_bf16 v[112:115], v[80:83], v[48:51], 0\n" \
  "v_mfma_f32_16x16x32_bf16 v[116:119], v[80:83], v[64:67], 0\n" \
  "v_mfma_f32_16x16x32_bf16 a[152:155], v[80:83], v[80:83], a[152:155]\n" \
  "s_waitcnt lgkmcnt(6)\n" \
  "v_mfma_f32_16x16x32_bf16 v[112:115], v[84:87], v[52:55], v[112:115]\n" \
  "v_mfma_f32_16x16x32_bf16 v[116:119], v[84:87], v[68:71], v[116:119]\n" \
  "v_cvt_pk_bf16_f32 v170, v120, v121\n" \
  "v_cvt_pk_bf16_f32 v171, v122, v123\n" \
  "v_cvt_pk_bf16_f32 v234, v124, v125\n" \
  "v_cvt_pk_bf16_f32 v235, v126, v127\n" \
  "v_mfma_f32_16x16x32_bf16 a[136:139], v[168:171], v[168:171], a[136:139]\n" \
  "s_nop 0\n" \
  "v_mfma_f32_16x16x32_bf16 a[140:143], v[232:235], v[232:235], a[140:143]\n" \
  "s_waitcnt lgkmcnt(5)\n" \
  "v_mfma_f32_16x16x32_bf16 v[112:115], v[88:91], v[56:59], v[112:115]\n" \
  "v_mfma_f32_16x16x32_bf16 v[116:119], v[88:91], v[72:75], v[116:119]\n" \
  "s_waitcnt lgkmcnt(4)\n" \
  "v_mfma_f32_16x16x32_bf16 v[112:115], v[92:95], v[60:63], v[112:115]\n" \
  "v_mfma_f32_16x16x32_bf16 v[116:119], v[92:95], v[76:79], v[116:119]\n" \
  "ds_read_b128 v[80:83], v44 offset:0\n" \
  "ds_read_b128 v[84:87], v45 offset:0\n" \
  "ds_read_b128 v[88:91], v46 offset:0\n" \
  "ds_read_b128 v[92:95], v47 offset:0\n" \
  "s_waitcnt lgkmcnt(7)\n" \
  "v_mfma_f32_16x16x32_bf16 v[120:123], v[96:99], v[48:51], 0\n" \
  "v_mfma_f32_16x16x32_bf16 v[124:127], v[96:99], v[64:67], 0\n" \
  "v_mfma_f32_16x16x32_bf16 a[152:155], v[96:99], v[96:99], a[152:155]\n" \
  "s_waitcnt lgkmcnt(6)\n" \
  "v_mfma_f32_16x16x32_bf16 v[120:123], v[100:103], v[52:55], v[120:123]\n" \
  "v_mfma_f32_16x16x32_bf16 v[124:127], v[100:103], v[68:71], v[124:127]\n" \
  "v_cvt_pk_bf16_f32 v172, v112, v113\n" \
  "v_cvt_pk_bf16_f32 v173, v114, v115\n" \
  "v_cvt_pk_bf16_f32 v236, v116, v117\n" \
  "v_cvt_pk_bf16_f32 v237, v118, v119\n" \
  "s_waitcnt lgkmcnt(5)\n" \
  "v_mfma_f32_16x16x32_bf16 v[120:123], v[104:107], v[56:59], v[120:123]\n" \
  "v_mfma_f32_16x16x32_bf16 v[124:127], v[104:107], v[72:75], v[124:127]\n" \
  "s_waitcnt lgkmcnt(4)\n" \
  "v_mfma_f32_16x16x32_bf16 v[120:123], v[108:111], v[60:63], v[120:123]\n" \
  "v_mfma_f32_16x16x32_bf16 v[124:127], v[108:111], v[76:79], v[124:127]\n" \
  "ds_read_b128 v[96:99], v44 offset:1024\n" \
  "ds_read_b128 v[100:103], v45 offset:1024\n" \
  "ds_read_b128 v[104:107], v46 offset:1024\n" \
  "ds_read_b128 v[108:111], v47 offset:1024\n" \
  "s_waitcnt lgkmcnt(7)\n" \
  "v_mfma_f32_16x16x32_bf16 v[112:115], v[80:83], v[48:51], 0\n" \
  "v_mfma_f32_16x16x32_bf16 v[116:119], v[80:83], v[64:67], 0\n" \
  "v_mfma_f32_16x16x32_bf16 a[152:155], v[80:83], v[80:83], a[152:155]\n" \
  "s_waitcnt lgkmcnt(6)\n" \
  "v_mfma_f32_16x16x32_bf16 v[112:115], v[84:87], v[52:55], v[112:115]\n" \
  "v_mfma_f32_16x16x32_bf16 v[116:119], v[84:87], v[68:71], v[116:119]\n" \
  "v_cvt_pk_bf16_f32 v174, v120, v121\n" \
  "v_cvt_pk_bf16_f32 v175, v122, v123\n" \
  "v_cvt_pk_bf16_f32 v238, v124, v125\n" \
  "v_cvt_pk_bf16_f32 v239, v126, v127\n" \
  "v_mfma_f32_16x16x32_bf16 a[136:139], v[172:175], v[172:175], a[136:139]\n" \
  "s_nop 0\n" \
  "v_mfma_f32_16x16x32_bf16 a[140:143], v[236:239], v[236:239], a[140:143]\n" \
  "s_waitcnt lgkmcnt(5)\n" \
  "v_mfma_f32_16x16x32_bf16 v[112:115], v[88:91], v[56:59], v[112:115]\n" \
  "v_mfma_f32_16x16x32_bf16 v[116:119], v[88:91], v[72:75], v[116:119]\n" \
  "s_waitcnt lgkmcnt(4)\n" \
  "v_mfma_f32_16x16x32_bf16 v[112:115], v[92:95], v[60:63], v[112:115]\n" \
  "v_mfma_f32_16x16x32_bf16 v[116:119], v[92:95], v[76:79], v[116:119]\n" \
  "ds_read_b128 v[80:83], v44 offset:8192\n" \
  "ds_read_b128 v[84:87], v45 offset:8192\n" \
  "ds_read_b128 v[88:91], v46 offset:8192\n" \
  "ds_read_b128 v[92:95], v47 offset:8192\n" \
  "s_waitcnt lgkmcnt(7)\n" \
  "v_mfma_f32_16x16x32_bf16 v[120:123], v[96:99], v[48:51], 0\n" \
  "v_mfma_f32_16x16x32_bf16 v[124:127], v[96:99], v[64:67], 0\n" \
  "v_mfma_f32_16x16x32_bf16 a[152:155], v[96:99], v[96:99], a[152:155]\n" \
  "s_waitcnt lgkmcnt(6)\n" \
  "v_mfma_f32_16x16x32_bf16 v[120:123], v[100:103], v[52:55], v[120:123]\n" \
  "v_mfma_f32_16x16x32_bf16 v[124:127], v[100:103], v[68:71], v[124:127]\n" \
  "v_cvt_pk_bf16_f32 v184, v112, v113\n" \
  "v_cvt_pk_bf16_f32 v185, v114, v115\n" \
  "v_cvt_pk_bf16_f32 v248, v116, v117\n" \
  "v_cvt_pk_bf16_f32 v249, v118, v119\n" \
  "s_waitcnt lgkmcnt(5)\n" \
  "v_mfma_f32_16x16x32_bf16 v[120:123], v[104:107], v[56:59], v[120:123]\n" \
  "v_mfma_f32_16x16x32_bf16 v[124:127], v[104:107], v[72:75], v[124:127]\n" \
  "s_waitcnt lgkmcnt(4)\n" \
  "v_mfma_f32_16x16x32_bf16 v[120:123], v[108:111], v[60:63], v[120:123]\n" \
  "v_mfma_f32_16x16x32_bf16 v[124:127], v[108:111], v[76:79], v[124:127]\n" \
  "ds_read_b128 v[96:99], v44 offset:9216\n" \
  "ds_read_b128 v[100:103], v45 offset:9216\n" \
  "ds_read_b128 v[104:107], v46 offset:9216\n" \
  "ds_read_b128 v[108:111], v47 offset:9216\n" \
  "s_waitcnt lgkmcnt(7)\n" \
  "v_mfma_f32_16x16x32_bf16 v[112:115], v[80:83], v[48:51], 0\n" \
  "v_mfma_f32_16x16x32_bf16 v[116:119], v[80:83], v[64:67], 0\n" \
  "v_mfma_f32_16x16x32_bf16 a[152:155], v[80:83], v[80:83], a[152:155]\n" \
  "s_waitcnt lgkmcnt(6)\n" \
  "v_mfma_f32_16x16x32_bf16 v[112:115], v[84:87], v[52:55], v[112:115]\n" \
  "v_mfma_f32_16x16x32_bf16 v[116:119], v[84:87], v[68:71], v[116:119]\n" \
  "v_cvt_pk_bf16_f32 v186, v120, v121\n" \
  "v_cvt_pk_bf16_f32 v187, v122, v123\n" \
  "v_cvt_pk_bf16_f32 v250, v124, v125\n" \
  "v_cvt_pk_bf16_f32 v251, v126, v127\n" \
  "v_mfma_f32_16x16x32_bf16 a[136:139], v[184:187], v[184:187], a[136:139]\n" \
  "s_nop 0\n" \
  "v_mfma_f32_16x16x32_bf16 a[140:143], v[248:251], v[248:251], a[140:143]\n" \
  "s_waitcnt lgkmcnt(5)\n" \
  "v_mfma_f32_16x16x32_bf16 v[112:115], v[88:91], v[56:59], v[112:115]\n" \
  "v_mfma_f32_16x16x32_bf16 v[116:119], v[88:91], v[72:75], v[116:119]\n" \
  "s_waitcnt lgkmcnt(4)\n" \
  "v_mfma_f32_16x16x32_bf16 v[112:115], v[92:95], v[60:63], v[112:115]\n" \
  "v_mfma_f32_16x16x32_bf16 v[116:119], v[92:95], v[76:79], v[116:119]\n" \
  "s_waitcnt lgkmcnt(3)\n" \
  "v_mfma_f32_16x16x32_bf16 v[120:123], v[96:99], v[48:51], 0\n" \
  "v_mfma_f32_16x16x32_bf16 v[124:127], v[96:99], v[64:67], 0\n" \
  "v_mfma_f32_16x16x32_bf16 a[152:155], v[96:99], v[96:99], a[152:155]\n" \
  "s_waitcnt lgkmcnt(2)\n" \
  "v_mfma_f32_16x16x32_bf16 v[120:123], v[100:103], v[52:55], v[120:123]\n" \
  "v_mfma_f32_16x16x32_bf16 v[124:127], v[100:103], v[68:71], v[124:127]\n" \
  "v_cvt_pk_bf16_f32 v188, v112, v113\n" \
  "v_cvt_pk_bf16_f32 v189, v114, v115\n" \
  "v_cvt_pk_bf16_f32 v252, v116, v117\n" \
  "v_cvt_pk_bf16_f32 v253, v118, v119\n" \
  "s_waitcnt lgkmcnt(1)\n" \
  "v_mfma_f32_16x16x32_bf16 v[120:123], v[104:107], v[56:59], v[120:123]\n" \
  "v_mfma_f32_16x16x32_bf16 v[124:127], v[104:107], v[72:75], v[124:127]\n" \
  "s_waitcnt lgkmcnt(0)\n" \
  "v_mfma_f32_16x16x32_bf16 v[120:123], v[108:111], v[60:63], v[120:123]\n" \
  "v_mfma_f32_16x16x32_bf16 v[124:127], v[108:111], v[76:79], v[124:127]\n" \
  "s_nop 6\n" \
  "v_cvt_pk_bf16_f32 v190, v120, v121\n" \
  "v_cvt_pk_bf16_f32 v191, v122, v123\n" \
  "v_cvt_pk_bf16_f32 v254, v124, v125\n" \
  "v_cvt_pk_bf16_f32 v255, v126, v127\n" \
  "v_mfma_f32_16x16x32_bf16 a[136:139], v[188:191], v[188:191], a[136:139]\n" \
  "s_nop 0\n" \
  "v_mfma_f32_16x16x32_bf16 a[140:143], v[252:255], v[252:255], a[140:143]\n" \
  "s_nop 7\n" \
  "s_nop 7\n" \
  "v_accvgpr_read_b32 v80, a136\n" \
  "v_accvgpr_read_b32 v81, a137\n" \
  "v_accvgpr_read_b32 v82, a138\n" \
  "v_accvgpr_read_b32 v83, a139\n" \
  "v_accvgpr_read_b32 v84, a140\n" \
  "v_accvgpr_read_b32 v85, a141\n" \
  "v_accvgpr_read_b32 v86, a142\n" \
  "v_accvgpr_read_b32 v87, a143\n" \
  "v_accvgpr_read_b32 v88, a144\n" \
  "v_accvgpr_read_b32 v89, a145\n" \
  "v_accvgpr_read_b32 v90, a146\n" \
  "v_accvgpr_read_b32 v91, a147\n" \
  "v_accvgpr_read_b32 v92, a148\n" \
  "v_accvgpr_read_b32 v93, a149\n" \
  "v_accvgpr_read_b32 v94, a150\n" \
  "v_accvgpr_read_b32 v95, a151\n" \
  "v_accvgpr_read_b32 v96, a152\n" \
  "v_accvgpr_read_b32 v97, a153\n" \
  "v_accvgpr_read_b32 v98, a154\n" \
  "v_accvgpr_read_b32 v99, a155\n" \
  "v_max_f32 v80, v80, v84\n" \
  "v_max_f32 v88, v88, v92\n" \
  "v_max_f32 v81, v81, v85\n" \
  "v_max_f32 v89, v89, v93\n" \
  "v_max_f32 v82, v82, v86\n" \
  "v_max_f32 v90, v90, v94\n" \
  "v_max_f32 v83, v83, v87\n" \
  "v_max_f32 v91, v91, v95\n" \
  "v_cmp_eq_u32 vcc, 1, v4\n" \
  "s_nop 1\n" \
  "v_cndmask_b32 v80, v80, v81, vcc\n" \
  "v_cndmask_b32 v88, v88, v89, vcc\n" \
  "v_cndmask_b32 v96, v96, v97, vcc\n" \
  "v_cmp_eq_u32 vcc, 2, v4\n" \
  "s_nop 1\n" \
  "v_cndmask_b32 v80, v80, v82, vcc\n" \
  "v_cndmask_b32 v88, v88, v90, vcc\n" \
  "v_cndmask_b32 v96, v96, v98, vcc\n" \
  "v_cmp_eq_u32 vcc, 3, v4\n" \
  "s_nop 1\n" \
  "v_cndmask_b32 v80, v80, v83, vcc\n" \
  "v_cndmask_b32 v88, v88, v91, vcc\n" \
  "v_cndmask_b32 v96, v96, v99, vcc\n" \
  "v_cmp_eq_u32 vcc, v2, v3\n" \
  "s_nop 1\n" \
  "v_cndmask_b32 v100, v13, v80, vcc\n" \
  "v_mov_b32 v5, v100\n" \
  "s_nop 1\n" \
  "v_permlane32_swap_b32 v100, v5\n" \
  "v_max_f32 v100, v100, v5\n" \
  "v_mov_b32 v5, v100\n" \
  "s_nop 1\n" \
  "v_permlane16_swap_b32 v100, v5\n" \
  "v_max_f32 v100, v100, v5\n" \
  "s_nop 1\n" \
  "v_max_f32_dpp v100, v100, v100 row_ror:8 row_mask:0xf bank_mask:0xf\n" \
  "s_nop 1\n" \
  "v_max_f32_dpp v100, v100, v100 row_ror:4 row_mask:0xf bank_mask:0xf\n" \
  "s_nop 1\n" \
  "v_max_f32_dpp v100, v100, v100 row_ror:2 row_mask:0xf bank_mask:0xf\n" \
  "s_nop 1\n" \
  "v_max_f32_dpp v100, v100, v100 row_ror:1 row_mask:0xf bank_mask:0xf\n" \
  "v_cndmask_b32 v101, v13, v96, vcc\n" \
  "v_mov_b32 v5, v101\n" \
  "s_nop 1\n" \
  "v_permlane32_swap_b32 v101, v5\n" \
  "v_max_f32 v101, v101, v5\n" \
  "v_mov_b32 v5, v101\n" \
  "s_nop 1\n" \
  "v_permlane16_swap_b32 v101, v5\n" \
  "v_max_f32 v101, v101, v5\n" \
  "s_nop 1\n" \
  "v_max_f32_dpp v101, v101, v101 row_ror:8 row_mask:0xf bank_mask:0xf\n" \
  "s_nop 1\n" \
  "v_max_f32_dpp v101, v101, v101 row_ror:4 row_mask:0xf bank_mask:0xf\n" \
  "s_nop 1\n" \
  "v_max_f32_dpp v101, v101, v101 row_ror:2 row_mask:0xf bank_mask:0xf\n" \
  "s_nop 1\n" \
  "v_max_f32_dpp v101, v101, v101 row_ror:1 row_mask:0xf bank_mask:0xf\n" \
  "v_cndmask_b32 v102, v13, v88, vcc\n" \
  "v_mov_b32 v5, v102\n" \
  "s_nop 1\n" \
  "v_permlane32_swap_b32 v102, v5\n" \
  "v_max_f32 v102, v102, v5\n" \
  "v_mov_b32 v5, v102\n" \
  "s_nop 1\n" \
  "v_permlane16_swap_b32 v102, v5\n" \
  "v_max_f32 v102, v102, v5\n" \
  "s_nop 1\n" \
  "v_max_f32_dpp v102, v102, v102 row_ror:8 row_mask:0xf bank_mask:0xf\n" \
  "s_nop 1\n" \
  "v_max_f32_dpp v102, v102, v102 row_ror:4 row_mask:0xf bank_mask:0xf\n" \
  "s_nop 1\n" \
  "v_max_f32_dpp v102, v102, v102 row_ror:2 row_mask:0xf bank_mask:0xf\n" \
  "s_nop 1\n" \
  "v_max_f32_dpp v102, v102, v102 row_ror:1 row_mask:0xf bank_mask:0xf\n" \
  "v_sqrt_f32 v100, v100\n" \
  "s_nop 0\n" \
  "v_mul_f32 v100, 0x3f8020c5, v100\n" \
  "v_sqrt_f32 v101, v101\n" \
  "s_nop 0\n" \
  "v_mul_f32 v101, 0x410020c5, v101\n" \
  "v_sqrt_f32 v102, v102\n" \
  "s_nop 0\n" \
  "v_mul_f32 v102, 0x3f8020c5, v102\n" \
  "v_mov_b32 v103, 1\n" \
  "s_lshl_b32 s58, s76, 4\n" \
  "s_add_u32 s58, s58, 131072\n" \
  "v_mov_b32 v5, s58\n" \
  "v_mov_b32 v6, 131072\n" \
  "ds_write_b128 v5, v[100:103] offset:0\n" \
  "s_add_u32 s64, s36, 4195328\n" \
  "s_addc_u32 s65, s37, 0\n" \
  "s_waitcnt vmcnt(0)\n" \
  "s_waitcnt lgkmcnt(0)\n" \
  "s_barrier\n" \
  "ds_read_b128 v[80:83], v6 offset:0\n" \
  "ds_read_b128 v[84:87], v6 offset:16\n" \
  "ds_read_b128 v[88:91], v6 offset:32\n" \
  "ds_read_b128 v[92:95], v6 offset:48\n" \
  "s_waitcnt lgkmcnt(2)\n" \
  "v_max_f32 v80, v80, v84\n" \
  "s_waitcnt lgkmcnt(0)\n" \
  "v_max3_f32 v80, v80, v88, v92\n" \
  "v_max_f32 v81, v81, v85\n" \
  "v_max3_f32 v81, v81, v89, v93\n" \
  "v_max_f32 v82, v82, v86\n" \
  "v_max3_f32 v82, v82, v90, v94\n" \
  "s_lshr_b32 s58, %[oWarm], 10\n" \
  "v_mov_b32 v5, s58\n" \
  "s_cmp_eq_u32 s76, 0\n" \
  "s_cselect_b64 exec, 1, 0\n" \
  "global_store_dwordx4 v5, v[80:83], s[64:65] sc1\n" \
  "s_mov_b64 exec, -1\n" \
  "s_mov_b64 s[40:41], %[pWk]\n" \
  "s_add_u32 m0, s45, 65536\n" \
  "s_add_u32 s58, s46, 0\n" \
  "buffer_load_dwordx4 v8, s[40:43], s58 offen lds\n" \
  "s_add_u32 m0, s45, 66560\n" \
  "s_add_u32 s58, s46, 32768\n" \
  "buffer_load_dwordx4 v8, s[40:43], s58 offen lds\n" \
  "s_add_u32 m0, s45, 67584\n" \
  "s_add_u32 s58, s46, 65536\n" \
  "buffer_load_dwordx4 v9, s[40:43], s58 offen lds\n" \
  "s_add_u32 m0, s45, 68608\n" \
  "s_add_u32 s58, s46, 98304\n" \
  "buffer_load_dwordx4 v9, s[40:43], s58 offen lds\n" \
  "s_add_u32 m0, s45, 69632\n" \
  "s_add_u32 s58, s46, 131072\n" \
  "buffer_load_dwordx4 v8, s[40:43], s58 offen lds\n" \
  "s_add_u32 m0, s45, 70656\n" \
  "s_add_u32 s58, s46, 163840\n" \
  "buffer_load_dwordx4 v8, s[40:43], s58 offen lds\n" \
  "s_add_u32 m0, s45, 71680\n" \
  "s_add_u32 s58, s46, 196608\n" \
  "buffer_load_dwordx4 v9, s[40:43], s58 offen lds\n" \
  "s_add_u32 m0, s45, 72704\n" \
  "s_add_u32 s58, s46, 229376\n" \
  "buffer_load_dwordx4 v9, s[40:43], s58 offen lds\n" \
  "s_add_u32 m0, s45, 73728\n" \
  "s_add_u32 s58, s46, 262144\n" \
  "buffer_load_dwordx4 v8, s[40:43], s58 offen lds\n" \
  "s_add_u32 m0, s45, 74752\n" \
  "s_add_u32 s58, s46, 294912\n" \
  "buffer_load_dwordx4 v8, s[40:43], s58 offen lds\n" \
  "s_add_u32 m0, s45, 75776\n" \
  "s_add_u32 s58, s46, 327680\n" \
  "buffer_load_dwordx4 v9, s[40:43], s58 offen lds\n" \
  "s_add_u32 m0, s45, 76800\n" \
  "s_add_u32 s58, s46, 360448\n" \
  "buffer_load_dwordx4 v9, s[40:43], s58 offen lds\n" \
  "s_add_u32 m0, s45, 77824\n" \
  "s_add_u32 s58, s46, 393216\n" \
  "buffer_load_dwordx4 v8, s[40:43], s58 offen lds\n" \
  "s_add_u32 m0, s45, 78848\n" \
  "s_add_u32 s58, s46, 425984\n" \
  "buffer_load_dwordx4 v8, s[40:43], s58 offen lds\n" \
  "s_add_u32 m0, s45, 79872\n" \
  "s_add_u32 s58, s46, 458752\n" \
  "buffer_load_dwordx4 v9, s[40:43], s58 offen lds\n" \
  "s_add_u32 m0, s45, 80896\n" \
  "s_add_u32 s58, s46, 491520\n" \
  "buffer_load_dwordx4 v9, s[40:43], s58 offen lds\n" \
  "ds_read_b64_tr_b16 a[8:9], v16 offset:0\n" \
  "ds_read_b64_tr_b16 a[10:11], v16 offset:1024\n" \
  "ds_read_b64_tr_b16 a[12:13], v16 offset:8192\n" \
  "ds_read_b64_tr_b16 a[14:15], v16 offset:9216\n" \
  "ds_read_b64_tr_b16 a[24:25], v17 offset:0\n" \
  "ds_read_b64_tr_b16 a[26:27], v17 offset:1024\n" \
  "ds_read_b64_tr_b16 a[28:29], v17 offset:8192\n" \
  "ds_read_b64_tr_b16 a[30:31], v17 offset:9216\n" \
  "ds_read_b64_tr_b16 a[40:41], v18 offset:0\n" \
  "ds_read_b64_tr_b16 a[42:43], v18 offset:1024\n" \
  "ds_read_b64_tr_b16 a[44:45], v18 offset:8192\n" \
  "ds_read_b64_tr_b16 a[46:47], v18 offset:9216\n" \
  "s_waitcnt lgkmcnt(4)\n" \
  "ds_read_b64_tr_b16 a[56:57], v19 offset:0\n" \
  "ds_read_b64_tr_b16 a[58:59], v19 offset:1024\n" \
  "ds_read_b64_tr_b16 a[60:61], v19 offset:8192\n" \
  "ds_read_b64_tr_b16 a[62:63], v19 offset:9216\n" \
  "ds_read_b64_tr_b16 a[72:73], v20 offset:0\n" \
  "ds_read_b64_tr_b16 a[74:75], v20 offset:1024\n" \
  "ds_read_b64_tr_b16 a[76:77], v20 offset:8192\n" \
  "ds_read_b64_tr_b16 a[78:79], v20 offset:9216\n" \
  "ds_read_b64_tr_b16 a[88:89], v21 offset:0\n" \
  "ds_read_b64_tr_b16 a[90:91], v21 offset:1024\n" \
  "ds_read_b64_tr_b16 a[92:93], v21 offset:8192\n" \
  "ds_read_b64_tr_b16 a[94:95], v21 offset:9216\n" \
  "s_waitcnt lgkmcnt(4)\n" \
  "ds_read_b64_tr_b16 a[104:105], v22 offset:0\n" \
  "ds_read_b64_tr_b16 a[106:107], v22 offset:1024\n" \
  "ds_read_b64_tr_b16 a[108:109], v22 offset:8192\n" \
  "ds_read_b64_tr_b16 a[110:111], v22 offset:9216\n" \
  "ds_read_b64_tr_b16 a[120:121], v23 offset:0\n" \
  "ds_read_b64_tr_b16 a[122:123], v23 offset:1024\n" \
  "ds_read_b64_tr_b16 a[124:125], v23 offset:8192\n" \
  "ds_read_b64_tr_b16 a[126:127], v23 offset:9216\n" \
  "s_waitcnt lgkmcnt(0)\n" \
  "s_waitcnt lgkmcnt(0)\n" \
  "s_barrier\n" \
  "s_mov_b64 s[40:41], %[pWk]\n" \
  "s_add_u32 m0, s45, 0\n" \
  "s_add_u32 s58, s46, 524288\n" \
  "buffer_load_dwordx4 v8, s[40:43], s58 offen lds\n" \
  "s_add_u32 m0, s45, 1024\n" \
  "s_add_u32 s58, s46, 557056\n" \
  "buffer_load_dwordx4 v8, s[40:43], s58 offen lds\n" \
  "s_add_u32 m0, s45, 2048\n" \
  "s_add_u32 s58, s46, 589824\n" \
  "buffer_load_dwordx4 v9, s[40:43], s58 offen lds\n" \
  "s_add_u32 m0, s45, 3072\n" \
  "s_add_u32 s58, s46, 622592\n" \
  "buffer_load_dwordx4 v9, s[40:43], s58 offen lds\n" \
  "s_add_u32 m0, s45, 4096\n" \
  "s_add_u32 s58, s46, 655360\n" \
  "buffer_load_dwordx4 v8, s[40:43], s58 offen lds\n" \
  "s_add_u32 m0, s45, 5120\n" \
  "s_add_u32 s58, s46, 688128\n" \
  "buffer_load_dwordx4 v8, s[40:43], s58 offen lds\n" \
  "s_add_u32 m0, s45, 6144\n" \
  "s_add_u32 s58, s46, 720896\n" \
  "buffer_load_dwordx4 v9, s[40:43], s58 offen lds\n" \
  "s_add_u32 m0, s45, 7168\n" \
  "s_add_u32 s58, s46, 753664\n" \
  "buffer_load_dwordx4 v9, s[40:43], s58 offen lds\n" \
  "s_add_u32 m0, s45, 8192\n" \
  "s_add_u32 s58, s46, 786432\n" \
  "buffer_load_dwordx4 v8, s[40:43], s58 offen lds\n" \
  "s_add_u32 m0, s45, 9216\n" \
  "s_add_u32 s58, s46, 819200\n" \
  "buffer_load_dwordx4 v8, s[40:43], s58 offen lds\n" \
  "s_add_u32 m0, s45, 10240\n" \
  "s_add_u32 s58, s46, 851968\n" \
  "buffer_load_dwordx4 v9, s[40:43], s58 offen lds\n" \
  "s_add_u32 m0, s45, 11264\n" \
  "s_add_u32 s58, s46, 884736\n" \
  "buffer_load_dwordx4 v9, s[40:43], s58 offen lds\n" \
  "s_add_u32 m0, s45, 12288\n" \
  "s_add_u32 s58, s46, 917504\n" \
  "buffer_load_dwordx4 v8, s[40:43], s58 offen lds\n" \
  "s_add_u32 m0, s45, 13312\n" \
  "s_add_u32 s58, s46, 950272\n" \
  "buffer_load_dwordx4 v8, s[40:43], s58 offen lds\n" \
  "s_add_u32 m0, s45, 14336\n" \
  "s_add_u32 s58, s46, 983040\n" \
  "buffer_load_dwordx4 v9, s[40:43], s58 offen lds\n" \
  "s_add_u32 m0, s45, 15360\n" \
  "s_add_u32 s58, s46, 1015808\n" \
  "buffer_load_dwordx4 v9, s[40:43], s58 offen lds\n" \
  "v_lshlrev_b32 v96, 4, v0\n" \
  "s_mov_b64 s[40:41], %[pMem0]\n" \
  "s_mov_b32 s42, 0x2000\n" \
  "s_mov_b32 s43, 0x00020000\n" \
  "v_mbcnt_lo_u32_b32 v32, -1, 0\n" \
  "v_mbcnt_hi_u32_b32 v32, -1, v32\n" \
  "v_bfe_u32 v34, v32, 1, 3\n" \
  "v_lshlrev_b32 v34, 10, v34\n" \
  "v_lshrrev_b32 v35, 4, v32\n" \
  "v_lshl_add_u32 v34, v35, 5, v34\n" \
  "v_and_b32 v35, 1, v32\n" \
  "v_lshl_add_u32 v34, v35, 2, v34\n" \
  "s_lshl_b32 s58, s76, 8\n" \
  "buffer_load_dword v40, v34, s[40:43], s58 offen offset:0\n" \
  "buffer_load_dword v41, v34, s[40:43], s58 offen offset:8\n" \
  "buffer_load_dword v42, v34, s[40:43], s58 offen offset:16\n" \
  "buffer_load_dword v43, v34, s[40:43], s58 offen offset:24\n" \
  "buffer_load_dword v44, v34, s[40:43], s58 offen offset:128\n" \
  "buffer_load_dword v45, v34, s[40:43], s58 offen offset:136\n" \
  "buffer_load_dword v46, v34, s[40:43], s58 offen offset:144\n" \
  "buffer_load_dword v47, v34, s[40:43], s58 offen offset:152\n" \
  "global_load_dwordx4 v[88:91], v96, s[64:65] offset:0 sc1\n" \
  "global_load_dwordx4 v[92:95], v96, s[64:65] offset:1024 sc1\n" \
  "s_waitcnt vmcnt(26)\n" \
  "s_waitcnt lgkmcnt(0)\n" \
  "s_barrier\n" \
  "ds_read_b64_tr_b16 a[128:129], v24 offset:0\n" \
  "ds_read_b64_tr_b16 a[130:131], v24 offset:1024\n" \
  "ds_read_b64_tr_b16 a[132:133], v24 offset:8192\n" \
  "ds_read_b64_tr_b16 a[134:135], v24 offset:9216\n" \
  "ds_read_b64_tr_b16 a[144:145], v25 offset:0\n" \
  "ds_read_b64_tr_b16 a[146:147], v25 offset:1024\n" \
  "ds_read_b64_tr_b16 a[148:149], v25 offset:8192\n" \
  "ds_read_b64_tr_b16 a[150:151], v25 offset:9216\n" \
  "ds_read_b64_tr_b16 a[160:161], v26 offset:0\n" \
  "ds_read_b64_tr_b16 a[162:163], v26 offset:1024\n" \
  "ds_read_b64_tr_b16 a[164:165], v26 offset:8192\n" \
  "ds_read_b64_tr_b16 a[166:167], v26 offset:9216\n" \
  "s_waitcnt lgkmcnt(4)\n" \
  "ds_read_b64_tr_b16 a[176:177], v27 offset:0\n" \
  "ds_read_b64_tr_b16 a[178:179], v27 offset:1024\n" \
  "ds_read_b64_tr_b16 a[180:181], v27 offset:8192\n" \
  "ds_read_b64_tr_b16 a[182:183], v27 offset:9216\n" \
  "ds_read_b64_tr_b16 a[192:193], v28 offset:0\n" \
  "ds_read_b64_tr_b16 a[194:195], v28 offset:1024\n" \
  "ds_read_b64_tr_b16 a[196:197], v28 offset:8192\n" \
  "ds_read_b64_tr_b16 a[198:199], v28 offset:9216\n" \
  "ds_read_b64_tr_b16 a[208:209], v29 offset:0\n" \
  "ds_read_b64_tr_b16 a[210:211], v29 offset:1024\n" \
  "ds_read_b64_tr_b16 a[212:213], v29 offset:8192\n" \
  "ds_read_b64_tr_b16 a[214:215], v29 offset:9216\n" \
  "s_waitcnt lgkmcnt(4)\n" \
  "ds_read_b64_tr_b16 a[224:225], v30 offset:0\n" \
  "ds_read_b64_tr_b16 a[226:227], v30 offset:1024\n" \
  "ds_read_b64_tr_b16 a[228:229], v30 offset:8192\n" \
  "ds_read_b64_tr_b16 a[230:231], v30 offset:9216\n" \
  "ds_read_b64_tr_b16 a[240:241], v31 offset:0\n" \
  "ds_read_b64_tr_b16 a[242:243], v31 offset:1024\n" \
  "ds_read_b64_tr_b16 a[244:245], v31 offset:8192\n" \
  "ds_read_b64_tr_b16 a[246:247], v31 offset:9216\n" \
  "s_waitcnt lgkmcnt(0)\n" \
  "s_waitcnt vmcnt(10)\n" \
  "s_waitcnt lgkmcnt(0)\n" \
  "s_barrier\n" \
  "ds_read_b64_tr_b16 a[136:137], v16 offset:0\n" \
  "ds_read_b64_tr_b16 a[138:139], v16 offset:1024\n" \
  "ds_read_b64_tr_b16 a[140:141], v16 offset:8192\n" \
  "ds_read_b64_tr_b16 a[142:143], v16 offset:9216\n" \
  "ds_read_b64_tr_b16 a[152:153], v17 offset:0\n" \
  "ds_read_b64_tr_b16 a[154:155], v17 offset:1024\n" \
  "ds_read_b64_tr_b16 a[156:157], v17 offset:8192\n" \
  "ds_read_b64_tr_b16 a[158:159], v17 offset:9216\n" \
  "ds_read_b64_tr_b16 a[168:169], v18 offset:0\n" \
  "ds_read_b64_tr_b16 a[170:171], v18 offset:1024\n" \
  "ds_read_b64_tr_b16 a[172:173], v18 offset:8192\n" \
  "ds_read_b64_tr_b16 a[174:175], v18 offset:9216\n" \
  "s_waitcnt lgkmcnt(4)\n" \
  "ds_read_b64_tr_b16 a[184:185], v19 offset:0\n" \
  "ds_read_b64_tr_b16 a[186:187], v19 offset:1024\n" \
  "ds_read_b64_tr_b16 a[188:189], v19 offset:8192\n" \
  "ds_read_b64_tr_b16 a[190:191], v19 offset:9216\n" \
  "ds_read_b64_tr_b16 a[200:201], v20 offset:0\n" \
  "ds_read_b64_tr_b16 a[202:203], v20 offset:1024\n" \
  "ds_read_b64_tr_b16 a[204:205], v20 offset:8192\n" \
  "ds_read_b64_tr_b16 a[206:207], v20 offset:9216\n" \
  "ds_read_b64_tr_b16 a[216:217], v21 offset:0\n" \
  "ds_read_b64_tr_b16 a[218:219], v21 offset:1024\n" \
  "ds_read_b64_tr_b16 a[220:221], v21 offset:8192\n" \
  "ds_read_b64_tr_b16 a[222:223], v21 offset:9216\n" \
  "s_waitcnt lgkmcnt(4)\n" \
  "ds_read_b64_tr_b16 a[232:233], v22 offset:0\n" \
  "ds_read_b64_tr_b16 a[234:235], v22 offset:1024\n" \
  "ds_read_b64_tr_b16 a[236:237], v22 offset:8192\n" \
  "ds_read_b64_tr_b16 a[238:239], v22 offset:9216\n" \
  "ds_read_b64_tr_b16 a[248:249], v23 offset:0\n" \
  "ds_read_b64_tr_b16 a[250:251], v23 offset:1024\n" \
  "ds_read_b64_tr_b16 a[252:253], v23 offset:8192\n" \
  "ds_read_b64_tr_b16 a[254:255], v23 offset:9216\n" \
  "s_waitcnt lgkmcnt(0)\n" \
  "s_waitcnt lgkmcnt(0)\n" \
  "s_barrier\n" \
  "v_mbcnt_lo_u32_b32 v64, -1, 0\n" \
  "v_mbcnt_hi_u32_b32 v64, -1, v64\n" \
  "v_and_b32 v65, 15, v64\n" \
  "v_lshrrev_b32 v66, 4, v64\n" \
  "v_lshlrev_b32 v0, 4, v64\n" \
  "v_lshrrev_b32 v77, 2, v65\n" \
  "v_and_b32 v78, 1, v65\n" \
  "v_bfe_u32 v76, v65, 1, 1\n" \
  "v_lshl_add_u32 v76, v76, 3, v78\n" \
  "v_lshl_add_u32 v76, v77, 1, v76\n" \
  "s_lshl_b32 s59, s47, 11\n" \
  "v_add_u32 v7, s59, v0\n" \
  "s_lshl_b32 s59, s48, 11\n" \
  "v_add_u32 v8, s59, v0\n" \
  "s_lshl_b32 s59, s49, 11\n" \
  "v_add_u32 v9, s59, v0\n" \
  "s_lshl_b32 s59, s50, 11\n" \
  "v_add_u32 v10, s59, v0\n" \
  "v_lshl_add_u32 v80, v66, 2, v77\n" \
  "v_lshlrev_b32 v2, 4, v80\n" \
  "v_lshl_add_u32 v2, v78, 3, v2\n" \
  "v_and_b32 v81, 3, v65\n" \
  "v_lshlrev_b32 v3, 5, v80\n" \
  "v_lshl_add_u32 v3, v81, 3, v3\n" \
  "s_lshl_b32 s58, s76, 5\n" \
  "v_add_u32 v4, s58, v65\n" \
  "v_lshlrev_b32 v4, 4, v4\n" \
  "v_lshl_add_u32 v4, v66, 2, v4\n" \
  "v_lshlrev_b32 v5, 1, v4\n" \
  "v_lshrrev_b32 v6, 3, v64\n" \
  "v_lshlrev_b32 v6, 8, v6\n" \
  "v_and_b32 v80, 7, v64\n" \
  "v_lshl_add_u32 v6, v80, 4, v6\n" \
  "s_lshl_b32 s58, s76, 13\n" \
  "v_add_u32 v6, s58, v6\n" \
  "v_and_b32 v82, 1, v66\n" \
  "v_lshrrev_b32 v83, 1, v66\n" \
  "v_lshlrev_b32 v11, 8, v77\n" \
  "v_lshl_add_u32 v11, v81, 5, v11\n" \
  "v_lshl_add_u32 v11, v82, 4, v11\n" \
  "v_lshl_add_u32 v11, v83, 3, v11\n" \
  "v_lshl_add_u32 v16, v83, 4, v76\n" \
  "v_lshlrev_b32 v16, 4, v16\n" \
  "v_lshl_add_u32 v16, v82, 3, v16\n" \
  "v_lshl_add_u32 v17, v77, 1, v78\n" \
  "v_lshlrev_b32 v17, 4, v17\n" \
  "v_lshl_add_u32 v19, v66, 4, v76\n" \
  "v_lshlrev_b32 v19, 4, v19\n" \
  "s_lshl_b32 s74, s76, 11\n" \
  "v_add_u32 v19, s74, v19\n" \
  "v_lshrrev_b32 v84, 3, v65\n" \
  "v_lshl_add_u32 v20, v84, 10, v0\n" \
  "v_lshl_add_u32 v21, v84, 2, v0\n" \
  "s_lshl_b32 s58, s76, 3\n" \
  "v_add_u32 v85, s58, v0\n" \
  "s_cmp_lt_u32 s76, 2\n" \
  "s_cselect_b64 vcc, -1, 0\n" \
  "s_nop 1\n" \
  "v_cndmask_b32 v21, v21, v85, vcc\n" \
  "v_cmp_lt_u32 s[70:71], 7, v65\n" \
  "v_cmp_eq_u32 vcc, 1, v78\n" \
  "v_mov_b32 v12, 0x44444444\n" \
  "v_mov_b32 v82, 0xeeeeeeee\n" \
  "s_nop 1\n" \
  "v_cndmask_b32 v12, v12, v82, vcc\n" \
  "s_lshl_b32 s58, s76, 4\n" \
  "s_lshl_b64 s[72:73], 0x3333, s58\n" \
  "v_mov_b32 v15, 0\n" \
  "s_mov_b32 s80, 0x43c00000\n" \
  "s_mov_b32 s57, 0\n" \
  "s_mov_b32 s56, 0\n" \
  "s_branch L_nw1_%=\n" \
  "L_nw_%=:\n" \
  "s_waitcnt vmcnt(1)\n" \
  "global_load_dwordx4 v[88:91], v0, s[64:65] offset:0 sc1\n" \
  "s_waitcnt vmcnt(1)\n" \
  "global_load_dwordx4 v[92:95], v0, s[64:65] offset:1024 sc1\n" \
  "L_nw1_%=:\n" \
  "s_waitcnt vmcnt(0)\n" \
  "v_and_b32 v91, v91, v95\n" \
  "v_cmp_eq_u32 vcc, 0, v91\n" \
  "s_nop 3\n" \
  "s_cbranch_vccz L_nwd_%=\n" \
  "s_sleep 12\n" \
  "s_add_u32 s56, s56, 1\n" \
  "s_cmp_lt_u32 s56, 400000\n" \
  "s_cbranch_scc1 L_nw_%=\n" \
  "s_mov_b32 s57, 1\n" \
  "L_nwd_%=:\n" \
  "v_max_f32 v88, v88, v92\n" \
  "v_mov_b32 v96, v88\n" \
  "s_nop 1\n" \
  "v_permlane32_swap_b32 v88, v96\n" \
  "v_max_f32 v88, v88, v96\n" \
  "v_mov_b32 v96, v88\n" \
  "s_nop 1\n" \
  "v_permlane16_swap_b32 v88, v96\n" \
  "v_max_f32 v88, v88, v96\n" \
  "s_nop 1\n" \
  "v_max_f32_dpp v88, v88, v88 row_ror:8 row_mask:0xf bank_mask:0xf\n" \
  "s_nop 1\n" \
  "v_max_f32_dpp v88, v88, v88 row_ror:4 row_mask:0xf bank_mask:0xf\n" \
  "s_nop 1\n" \
  "v_max_f32_dpp v88, v88, v88 row_ror:2 row_mask:0xf bank_mask:0xf\n" \
  "s_nop 1\n" \
  "v_max_f32_dpp v88, v88, v88 row_ror:1 row_mask:0xf bank_mask:0xf\n" \
  "v_max_f32 v89, v89, v93\n" \
  "v_mov_b32 v96, v89\n" \
  "s_nop 1\n" \
  "v_permlane32_swap_b32 v89, v96\n" \
  "v_max_f32 v89, v89, v96\n" \
  "v_mov_b32 v96, v89\n" \
  "s_nop 1\n" \
  "v_permlane16_swap_b32 v89, v96\n" \
  "v_max_f32 v89, v89, v96\n" \
  "s_nop 1\n" \
  "v_max_f32_dpp v89, v89, v89 row_ror:8 row_mask:0xf bank_mask:0xf\n" \
  "s_nop 1\n" \
  "v_max_f32_dpp v89, v89, v89 row_ror:4 row_mask:0xf bank_mask:0xf\n" \
  "s_nop 1\n" \
  "v_max_f32_dpp v89, v89, v89 row_ror:2 row_mask:0xf bank_mask:0xf\n" \
  "s_nop 1\n" \
  "v_max_f32_dpp v89, v89, v89 row_ror:1 row_mask:0xf bank_mask:0xf\n" \
  "v_max_f32 v90, v90, v94\n" \
  "v_mov_b32 v96, v90\n" \
  "s_nop 1\n" \
  "v_permlane32_swap_b32 v90, v96\n" \
  "v_max_f32 v90, v90, v96\n" \
  "v_mov_b32 v96, v90\n" \
  "s_nop 1\n" \
  "v_permlane16_swap_b32 v90, v96\n" \
  "v_max_f32 v90, v90, v96\n" \
  "s_nop 1\n" \
  "v_max_f32_dpp v90, v90, v90 row_ror:8 row_mask:0xf bank_mask:0xf\n" \
  "s_nop 1\n" \
  "v_max_f32_dpp v90, v90, v90 row_ror:4 row_mask:0xf bank_mask:0xf\n" \
  "s_nop 1\n" \
  "v_max_f32_dpp v90, v90, v90 row_ror:2 row_mask:0xf bank_mask:0xf\n" \
  "s_nop 1\n" \
  "v_max_f32_dpp v90, v90, v90 row_ror:1 row_mask:0xf bank_mask:0xf\n" \
  "v_mul_f32 v82, v89, v90\n" \
  "v_mul_f32 v82, 0x3c1a33cd, v82\n" \
  "v_mov_b32 v83, 0x3f18ff66\n" \
  "v_fma_f32 v82, v88, v83, v82\n" \
  "s_nop 0\n" \
  "v_readfirstlane_b32 s77, v82\n" \
  "v_mul_f32 v82, 0x42040000, v82\n" \
  "v_bfe_u32 v82, v82, 23, 8\n" \
  "v_add_u32 v82, -19, v82\n" \
  "v_max_i32 v82, 72, v82\n" \
  "v_min_i32 v82, 182, v82\n" \
  "v_mov_b32 v84, 0\n" \
  "v_mov_b32 v85, 0\n" \
  "v_mov_b32 v86, 0\n" \
  "v_mov_b32 v87, 0\n" \
  "ds_write_b128 v0, v[84:87] offset:14336\n" \
  "ds_write_b128 v0, v[84:87] offset:15360\n" \
  "ds_write_b128 v0, v[84:87] offset:16384\n" \
  "ds_write_b128 v0, v[84:87] offset:17408\n" \
  "ds_write_b128 v0, v[84:87] offset:18432\n" \
  "s_waitcnt vmcnt(0)\n" \
  "v_readfirstlane_b32 s63, v82\n" \
  "s_mov_b32 s56, 0\n" \
  "s_mov_b32 s78, %[l0]\n" \
  "s_branch L_layer2_%=\n" \
  "s_nop 7\n" \
  "s_nop 1\n" \
  "L_layer_%=:\n" \
  "s_mov_b64 s[40:41], %[pMem0]\n" \
  "s_mov_b32 s42, 0x2000\n" \
  "s_mov_b32 s43, 0x00020000\n" \
  "v_mbcnt_lo_u32_b32 v32, -1, 0\n" \
  "v_mbcnt_hi_u32_b32 v32, -1, v32\n" \
  "v_bfe_u32 v34, v32, 1, 3\n" \
  "v_lshlrev_b32 v34, 10, v34\n" \
  "v_lshrrev_b32 v35, 4, v32\n" \
  "v_lshl_add_u32 v34, v35, 5, v34\n" \
  "v_and_b32 v35, 1, v32\n" \
  "v_lshl_add_u32 v34, v35, 2, v34\n" \
  "s_lshl_b32 s58, s76, 8\n" \
  "buffer_load_dword v40, v34, s[40:43], s58 offen offset:0\n" \
  "buffer_load_dword v41, v34, s[40:43], s58 offen offset:8\n" \
  "buffer_load_dword v42, v34, s[40:43], s58 offen offset:16\n" \
  "buffer_load_dword v43, v34, s[40:43], s58 offen offset:24\n" \
  "buffer_load_dword v44, v34, s[40:43], s58 offen offset:128\n" \
  "buffer_load_dword v45, v34, s[40:43], s58 offen offset:136\n" \
  "buffer_load_dword v46, v34, s[40:43], s58 offen offset:144\n" \
  "buffer_load_dword v47, v34, s[40:43], s58 offen offset:152\n" \
  "s_lshl_b32 s45, s78, 21\n" \
  "v_lshrrev_b32 v33, 4, v32\n" \
  "v_lshlrev_b32 v33, 8, v33\n" \
  "v_and_b32 v36, 15, v32\n" \
  "v_lshl_add_u32 v33, v36, 3, v33\n" \
  "v_mov_b32 v38, 0\n" \
  "v_mov_b32 v39, 0\n" \
  "s_lshl_b32 s58, s76, 10\n" \
  "s_add_u32 s58, s58, %[oWarm]\n" \
  "s_add_u32 s58, s58, s45\n" \
  "v_add_u32 v33, s58, v33\n" \
  "global_atomic_add_x2 v33, v[38:39], s[36:37] offset:0\n" \
  "v_add_u32 v33, 4096, v33\n" \
  "global_atomic_add_x2 v33, v[38:39], s[36:37] offset:0\n" \
  "v_add_u32 v33, 4096, v33\n" \
  "global_atomic_add_x2 v33, v[38:39], s[36:37] offset:0\n" \
  "v_add_u32 v33, 4096, v33\n" \
  "global_atomic_add_x2 v33, v[38:39], s[36:37] offset:0\n" \
  "s_nop 7\n" \
  "s_nop 1\n" \
  "L_layer2_%=:\n" \
  "s_mov_b32 s44, 0\n" \
  "s_mov_b32 s81, s76\n" \
  "s_lshl_b32 s45, s78, 21\n" \
  "s_add_u32 s58, s45, %[oAt]\n" \
  "s_add_u32 s64, s36, s58\n" \
  "s_addc_u32 s65, s37, 0\n" \
  "s_lshl_b32 s88, s78, 24\n" \
  "s_add_u32 s88, s88, %[oKx]\n" \
  "s_mov_b32 s47, s88\n" \
  "s_mov_b32 s48, %[oU]\n" \
  "s_cmp_eq_u32 s78, 0\n" \
  "s_cselect_b32 s58, 0xa300000, 0\n" \
  "s_sub_u32 s50, %[oY], s58\n" \
  "s_sub_u32 s51, %[oKm], s58\n" \
  "s_mul_i32 s58, s78, 294912\n" \
  "s_add_u32 s89, s58, %[oFl]\n" \
  "s_mov_b32 s49, s89\n" \
  "s_add_u32 s58, s58, %[oPb]\n" \
  "s_sub_u32 s58, s58, 1024\n" \
  "v_mov_b32 v14, 0\n" \
  "s_add_u32 s66, s36, s58\n" \
  "s_addc_u32 s67, s37, 0\n" \
  "s_mov_b32 s75, 20480\n" \
  "s_mov_b32 s86, 36864\n" \
  "s_mov_b32 s87, 53248\n" \
  "s_mul_i32 s90, s78, 294912\n" \
  "s_add_u32 s90, s90, 4427776\n" \
  "v_mov_b32 v64, 0\n" \
  "v_mov_b32 v65, 0\n" \
  "v_mov_b32 v66, 0\n" \
  "v_mov_b32 v67, 0\n" \
  "v_mov_b32 v68, 0\n" \
  "v_mov_b32 v69, 0\n" \
  "v_mov_b32 v70, 0\n" \
  "v_mov_b32 v71, 0\n" \
  "v_mov_b32 v72, 0\n" \
  "v_mov_b32 v73, 0\n" \
  "v_mov_b32 v74, 0\n" \
  "v_mov_b32 v75, 0\n" \
  "v_mov_b32 v76, 0\n" \
  "v_mov_b32 v77, 0\n" \
  "v_mov_b32 v78, 0\n" \
  "v_mov_b32 v79, 0\n" \
  "v_mov_b32 v80, 0\n" \
  "v_mov_b32 v81, 0\n" \
  "v_mov_b32 v82, 0\n" \
  "v_mov_b32 v83, 0\n" \
  "v_mov_b32 v84, 0\n" \
  "v_mov_b32 v85, 0\n" \
  "v_mov_b32 v86, 0\n" \
  "v_mov_b32 v87, 0\n" \
  "s_waitcnt vmcnt(0)\n" \
  "v_mov_b32 v32, v40\n" \
  "v_lshlrev_b32 v88, 16, v40\n" \
  "v_and_b32 v89, 0xffff0000, v40\n" \
  "v_mov_b32 v33, v41\n" \
  "v_lshlrev_b32 v90, 16, v41\n" \
  "v_and_b32 v91, 0xffff0000, v41\n" \
  "v_mov_b32 v34, v42\n" \
  "v_lshlrev_b32 v92, 16, v42\n" \
  "v_and_b32 v93, 0xffff0000, v42\n" \
  "v_mov_b32 v35, v43\n" \
  "v_lshlrev_b32 v94, 16, v43\n" \
  "v_and_b32 v95, 0xffff0000, v43\n" \
  "v_mov_b32 v36, v44\n" \
  "v_lshlrev_b32 v96, 16, v44\n" \
  "v_and_b32 v97, 0xffff0000, v44\n" \
  "v_mov_b32 v37, v45\n" \
  "v_lshlrev_b32 v98, 16, v45\n" \
  "v_and_b32 v99, 0xffff0000, v45\n" \
  "v_mov_b32 v38, v46\n" \
  "v_lshlrev_b32 v100, 16, v46\n" \
  "v_and_b32 v101, 0xffff0000, v46\n" \
  "v_mov_b32 v39, v47\n" \
  "v_lshlrev_b32 v102, 16, v47\n" \
  "v_and_b32 v103, 0xffff0000, v47\n" \
  "s_mov_b32 s79, 127\n" \
  "s_sub_u32 s58, s79, 127\n" \
  "v_cvt_f32_i32 v22, s58\n" \
  "s_lshl_b32 s61, s79, 23\n" \
  "s_lshl_b32 s58, s58, 23\n" \
  "s_add_u32 s52, s58, 0x3e0293ee\n" \
  "s_add_u32 s53, s52, s58\n" \
  "s_sub_u32 s54, 0x3fba144d, s58\n" \
  "s_min_u32 s54, s54, 0x4c600000\n" \
  "s_sub_u32 s55, 0x439cd760, s58\n" \
  "s_sub_u32 s59, s55, s58\n" \
  "s_cmp_lt_u32 s76, 2\n" \
  "s_cselect_b32 s55, s55, s59\n" \
  "s_mov_b32 s62, s63\n" \
  "s_add_u32 s58, s62, 15\n" \
  "s_mul_i32 s59, s58, 0x800080\n" \
  "v_mov_b32 v26, s59\n" \
  "v_mov_b32 v27, s59\n" \
  "v_mov_b32 v28, s59\n" \
  "v_mov_b32 v29, s59\n" \
  "s_add_u32 s58, s58, 127\n" \
  "s_sub_u32 s58, s58, s79\n" \
  "s_mul_i32 s59, s58, 0x800080\n" \
  "v_mov_b32 v30, s59\n" \
  "v_mov_b32 v31, s59\n" \
  "s_and_b32 s58, s76, 1\n" \
  "s_lshl_b32 s58, s58, 12\n" \
  "s_add_u32 s58, s58, s75\n" \
  "v_add_u32 v1, s58, v0\n" \
  "v_add_u32 v18, s75, v19\n" \
  "s_waitcnt lgkmcnt(0)\n" \
  "s_barrier\n" \
  "ds_write_b128 v7, v[32:35] offset:0\n" \
  "ds_write_b128 v7, v[36:39] offset:1024\n" \
  "v_max3_f32 v23, |v88|, |v89|, |v90|\n" \
  "v_max3_f32 v23, v23, |v91|, |v92|\n" \
  "v_max3_f32 v23, v23, |v93|, |v94|\n" \
  "v_max_f32 v23, v23, |v95|\n" \
  "v_mov_b32 v24, v23\n" \
  "v_max3_f32 v23, |v96|, |v97|, |v98|\n" \
  "v_max3_f32 v23, v23, |v99|, |v100|\n" \
  "v_max3_f32 v23, v23, |v101|, |v102|\n" \
  "v_max_f32 v23, v23, |v103|\n" \
  "v_max_f32 v23, v23, v24\n" \
  "v_cmp_lt_f32 vcc, s54, v23\n" \
  "s_nop 3\n" \
  "s_cbranch_vccz L_nb0_%=\n" \
  "v_mov_b32 v24, v23\n" \
  "v_mov_b32 v25, v24\n" \
  "s_nop 1\n" \
  "v_permlane32_swap_b32 v24, v25\n" \
  "v_max_f32 v24, v24, v25\n" \
  "v_mov_b32 v25, v24\n" \
  "s_nop 1\n" \
  "v_permlane16_swap_b32 v24, v25\n" \
  "v_max_f32 v24, v24, v25\n" \
  "s_nop 1\n" \
  "v_max_f32_dpp v24, v24, v24 row_ror:8 row_mask:0xf bank_mask:0xf\n" \
  "s_nop 1\n" \
  "v_max_f32_dpp v24, v24, v24 row_ror:4 row_mask:0xf bank_mask:0xf\n" \
  "s_nop 1\n" \
  "v_max_f32_dpp v24, v24, v24 row_ror:2 row_mask:0xf bank_mask:0xf\n" \
  "s_nop 1\n" \
  "v_max_f32_dpp v24, v24, v24 row_ror:1 row_mask:0xf bank_mask:0xf\n" \
  "s_nop 3\n" \
  "s_mov_b64 exec, 1\n" \
  "ds_max_u32 v15, v24 offset:14336\n" \
  "s_mov_b64 exec, -1\n" \
  "s_waitcnt lgkmcnt(0)\n" \
  "s_nop 7\n" \
  "s_nop 1\n" \
  "L_nb0_%=:\n" \
  "s_waitcnt lgkmcnt(0)\n" \
  "s_barrier\n" \
  "v_smfmac_f32_16x16x64_bf16 v[64:67], v[32:35], a[0:7], v12\n" \
  "v_smfmac_f32_16x16x64_bf16 v[68:71], v[32:35], a[64:71], v12\n" \
  "v_smfmac_f32_16x16x64_bf16 v[72:75], v[32:35], a[128:135], v12\n" \
  "v_smfmac_f32_16x16x64_bf16 v[76:79], v[32:35], a[192:199], v12\n" \
  "v_smfmac_f32_16x16x64_bf16 v[64:67], v[36:39], a[8:15], v12\n" \
  "v_smfmac_f32_16x16x64_bf16 v[68:71], v[36:39], a[72:79], v12\n" \
  "ds_read_b128 v[40:43], v8 offset:0\n" \
  "ds_read_b128 v[44:47], v8 offset:1024\n" \
  "ds_read_b128 v[48:51], v9 offset:0\n" \
  "ds_read_b128 v[52:55], v9 offset:1024\n" \
  "ds_read_b128 v[56:59], v10 offset:0\n" \
  "ds_read_b128 v[60:63], v10 offset:1024\n" \
  "v_smfmac_f32_16x16x64_bf16 v[72:75], v[36:39], a[136:143], v12\n" \
  "v_smfmac_f32_16x16x64_bf16 v[76:79], v[36:39], a[200:207], v12\n" \
  "v_smfmac_f32_16x16x64_bf16 v[80:83], v[32:35], v[128:135], v12\n" \
  "v_smfmac_f32_16x16x64_bf16 v[84:87], v[32:35], v[192:199], v12\n" \
  "v_smfmac_f32_16x16x64_bf16 v[80:83], v[36:39], v[136:143], v12\n" \
  "v_smfmac_f32_16x16x64_bf16 v[84:87], v[36:39], v[200:207], v12\n" \
  "s_waitcnt lgkmcnt(5)\n" \
  "v_smfmac_f32_16x16x64_bf16 v[64:67], v[40:43], a[16:23], v12\n" \
  "v_smfmac_f32_16x16x64_bf16 v[68:71], v[40:43], a[80:87], v12\n" \
  "s_waitcnt lgkmcnt(4)\n" \
  "v_smfmac_f32_16x16x64_bf16 v[64:67], v[44:47], a[24:31], v12\n" \
  "v_smfmac_f32_16x16x64_bf16 v[68:71], v[44:47], a[88:95], v12\n" \
  "v_smfmac_f32_16x16x64_bf16 v[72:75], v[40:43], a[144:151], v12\n" \
  "v_smfmac_f32_16x16x64_bf16 v[76:79], v[40:43], a[208:215], v12\n" \
  "s_waitcnt lgkmcnt(3)\n" \
  "v_smfmac_f32_16x16x64_bf16 v[64:67], v[48:51], a[32:39], v12\n" \
  "v_smfmac_f32_16x16x64_bf16 v[68:71], v[48:51], a[96:103], v12\n" \
  "v_smfmac_f32_16x16x64_bf16 v[72:75], v[44:47], a[152:159], v12\n" \
  "v_smfmac_f32_16x16x64_bf16 v[76:79], v[44:47], a[216:223], v12\n" \
  "s_waitcnt lgkmcnt(2)\n" \
  "v_smfmac_f32_16x16x64_bf16 v[64:67], v[52:55], a[40:47], v12\n" \
  "v_smfmac_f32_16x16x64_bf16 v[68:71], v[52:55], a[104:111], v12\n" \
  "s_waitcnt lgkmcnt(1)\n" \
  "v_smfmac_f32_16x16x64_bf16 v[64:67], v[56:59], a[48:55], v12\n" \
  "v_smfmac_f32_16x16x64_bf16 v[68:71], v[56:59], a[112:119], v12\n" \
  "v_smfmac_f32_16x16x64_bf16 v[72:75], v[48:51], a[160:167], v12\n" \
  "v_smfmac_f32_16x16x64_bf16 v[76:79], v[48:51], a[224:231], v12\n" \
  "s_waitcnt lgkmcnt(0)\n" \
  "v_smfmac_f32_16x16x64_bf16 v[64:67], v[60:63], a[56:63], v12\n" \
  "v_smfmac_f32_16x16x64_bf16 v[68:71], v[60:63], a[120:127], v12\n" \
  "v_smfmac_f32_16x16x64_bf16 v[72:75], v[52:55], a[168:175], v12\n" \
  "v_smfmac_f32_16x16x64_bf16 v[76:79], v[52:55], a[232:239], v12\n" \
  "s_nop 4\n" \
  "v_add_f32 v64, v64, v65\n" \
  "v_add_f32 v66, v66, v67\n" \
  "v_smfmac_f32_16x16x64_bf16 v[72:75], v[56:59], a[176:183], v12\n" \
  "v_add_f32 v68, v68, v69\n" \
  "v_add_f32 v70, v70, v71\n" \
  "v_smfmac_f32_16x16x64_bf16 v[76:79], v[56:59], a[240:247], v12\n" \
  "v_cvt_pk_bf16_f32 v24, v64, v66\n" \
  "v_cvt_pk_bf16_f32 v25, v68, v70\n" \
  "v_smfmac_f32_16x16x64_bf16 v[72:75], v[60:63], a[184:191], v12\n" \
  "ds_write2st64_b32 v4, v24, v25 offset0:32 offset1:33\n" \
  "v_smfmac_f32_16x16x64_bf16 v[76:79], v[60:63], a[248:255], v12\n" \
  "v_smfmac_f32_16x16x64_bf16 v[80:83], v[40:43], v[144:151], v12\n" \
  "v_smfmac_f32_16x16x64_bf16 v[84:87], v[40:43], v[208:215], v12\n" \
  "s_nop 3\n" \
  "v_add_f32 v72, v72, v73\n" \
  "v_add_f32 v74, v74, v75\n" \
  "v_add_f32 v76, v76, v77\n" \
  "v_smfmac_f32_16x16x64_bf16 v[80:83], v[44:47], v[152:159], v12\n" \
  "v_add_f32 v78, v78, v79\n" \
  "v_cvt_pk_bf16_f32 v24, v72, v74\n" \
  "v_cvt_pk_bf16_f32 v25, v76, v78\n" \
  "v_smfmac_f32_16x16x64_bf16 v[84:87], v[44:47], v[216:223], v12\n" \
  "ds_write2st64_b32 v5, v24, v25 offset0:40 offset1:42\n" \
  "s_waitcnt lgkmcnt(0)\n" \
  "v_min_u32 v90, 32, v0\n" \
  "v_lshlrev_b32 v90, 7, v90\n" \
  "s_cmp_eq_u32 s76, 0\n" \
  "s_cbranch_scc0 L_fwe_%=\n" \
  "L_fwi_%=:\n" \
  "buffer_load_dword v25, v15, s[36:39], s49 offen offset:0 sc1\n" \
  "s_waitcnt vmcnt(0)\n" \
  "v_and_b32 v25, 255, v25\n" \
  "v_cmp_eq_u32 vcc, 8, v25\n" \
  "s_nop 3\n" \
  "s_cbranch_vccnz L_fwdi_%=\n" \
  "s_cmp_lg_u32 s57, 0\n" \
  "s_cbranch_scc1 L_fwdi_%=\n" \
  "s_sleep 6\n" \
  "s_add_u32 s56, s56, 1\n" \
  "s_cmp_lt_u32 s56, 400000\n" \
  "s_cbranch_scc1 L_fwi_%=\n" \
  "s_mov_b32 s57, 1\n" \
  "L_fwdi_%=:\n" \
  "s_mov_b32 s56, 0\n" \
  "L_fwe_%=:\n" \
  "s_barrier\n" \
  "s_add_u32 m0, s74, s75\n" \
  "s_nop 0\n" \
  "buffer_load_dwordx4 v0, s[36:39], s47 offen offset:0 sc1 lds\n" \
  "buffer_load_dwordx4 v0, s[36:39], s47 offen offset:1024 sc1 lds\n" \
  "s_add_u32 m0, m0, 8192\n" \
  "s_nop 0\n" \
  "buffer_load_dwordx4 v0, s[36:39], s48 offen offset:0 sc1 lds\n" \
  "buffer_load_dwordx4 v0, s[36:39], s48 offen offset:1024 sc1 lds\n" \
  "buffer_load_dword v25, v90, s[36:39], s49 offen offset:0 sc1\n" \
  "s_add_u32 s47, s47, 262144\n" \
  "s_add_u32 s48, s48, 1048576\n" \
  "s_add_u32 s49, s49, 4096\n" \
  "s_waitcnt vmcnt(0)\n" \
  "s_waitcnt lgkmcnt(0)\n" \
  "s_barrier\n" \
  "ds_read_b128 v[104:107], v1 offset:0\n" \
  "ds_read_b128 v[108:111], v1 offset:1024\n" \
  "ds_read_b128 v[112:115], v1 offset:2048\n" \
  "ds_read_b128 v[116:119], v1 offset:3072\n" \
  "ds_read_b128 v[120:123], v18 offset:8192\n" \
  "ds_read_b128 v[124:127], v18 offset:9216\n" \
  "s_memrealtime s[40:41]\n" \
  "v_and_b32 v25, 255, v25\n" \
  "v_cmp_eq_u32 vcc, 8, v25\n" \
  "v_readlane_b32 s59, v25, 2\n" \
  "s_add_u32 s58, s89, 2048\n" \
  "buffer_load_dword v23, v15, s[36:39], s58 offen offset:0 sc1\n" \
  "s_nop 1\n" \
  "v_mov_b32 v13, s59\n" \
  "s_bitcmp0_b32 vcc_lo, 1\n" \
  "s_cselect_b32 s68, 1, 0\n" \
  "s_cbranch_scc1 L_nb1_%=\n" \
  "s_add_u32 m0, s74, s86\n" \
  "s_nop 0\n" \
  "buffer_load_dwordx4 v0, s[36:39], s47 offen offset:0 sc1 lds\n" \
  "buffer_load_dwordx4 v0, s[36:39], s47 offen offset:1024 sc1 lds\n" \
  "s_add_u32 m0, m0, 8192\n" \
  "s_nop 0\n" \
  "buffer_load_dwordx4 v0, s[36:39], s48 offen offset:0 sc1 lds\n" \
  "buffer_load_dwordx4 v0, s[36:39], s48 offen offset:1024 sc1 lds\n" \
  "L_nb1_%=:\n" \
  "s_add_u32 s47, s47, 262144\n" \
  "s_add_u32 s48, s48, 1048576\n" \
  "s_bitcmp0_b32 vcc_lo, 2\n" \
  "s_cbranch_scc1 L_nb2_%=\n" \
  "s_add_u32 m0, s74, s87\n" \
  "s_nop 0\n" \
  "buffer_load_dwordx4 v0, s[36:39], s47 offen offset:0 sc1 lds\n" \
  "buffer_load_dwordx4 v0, s[36:39], s47 offen offset:1024 sc1 lds\n" \
  "s_add_u32 m0, m0, 8192\n" \
  "s_nop 0\n" \
  "buffer_load_dwordx4 v0, s[36:39], s48 offen offset:0 sc1 lds\n" \
  "buffer_load_dwordx4 v0, s[36:39], s48 offen offset:1024 sc1 lds\n" \
  "L_nb2_%=:\n" \
  "s_and_b32 s58, %[h], 7\n" \
  "s_lshl_b32 s58, s58, 7\n" \
  "s_add_u32 s58, s58, s90\n" \
  "v_mov_b32 v88, s58\n" \
  "v_mov_b32 v91, 1\n" \
  "s_waitcnt lgkmcnt(0)\n" \
  "s_mov_b32 s91, s40\n" \
  "v_mov_b32 v89, s40\n" \
  "s_cmp_eq_u32 s76, 3\n" \
  "s_cselect_b64 exec, 1, 0\n" \
  "global_atomic_umax v88, v89, s[36:37] offset:0\n" \
  "s_mov_b64 exec, -1\n" \
  "s_nop 1\n" \
  "s_cbranch_scc1 L_x12I_%=\n" \
  "s_cmp_eq_u32 s76, 2\n" \
  "s_cbranch_scc1 L_x12M_%=\n" \
  "s_branch L_x12X_%=\n" \
  ".p2align 6\n" \
  "s_nop 0\n" \
  "s_nop 0\n" \
  "s_nop 0\n" \
  "s_nop 0\n" \
  "s_nop 0\n" \
  "s_nop 0\n" \
  "s_nop 0\n" \
  "s_nop 0\n" \
  "s_nop 0\n" \
  "s_nop 0\n" \
  "s_nop 0\n" \
  "s_nop 0\n" \
  "s_nop 0\n" \
  "s_nop 7\n" \
  "s_nop 1\n" \
  "L_loop_%=:\n" \
  "s_cmp_eq_u32 s44, 1\n" \
  "s_cbranch_scc1 L_first_%=\n" \
  "L_firstr_%=:\n" \
  "ds_read_b128 v[104:107], v1 offset:0\n" \
  "ds_read_b128 v[108:111], v1 offset:1024\n" \
  "ds_read_b128 v[112:115], v1 offset:2048\n" \
  "ds_read_b128 v[116:119], v1 offset:3072\n" \
  "ds_read_b128 v[120:123], v18 offset:8192\n" \
  "ds_read_b128 v[124:127], v18 offset:9216\n" \
  "v_mov_b32 v64, 0\n" \
  "v_mov_b32 v65, 0\n" \
  "v_mov_b32 v66, 0\n" \
  "v_mov_b32 v67, 0\n" \
  "v_mov_b32 v68, 0\n" \
  "v_mov_b32 v69, 0\n" \
  "v_mov_b32 v70, 0\n" \
  "v_mov_b32 v71, 0\n" \
  "v_mov_b32 v72, 0\n" \
  "v_mov_b32 v73, 0\n" \
  "v_mov_b32 v74, 0\n" \
  "v_mov_b32 v75, 0\n" \
  "v_mov_b32 v76, 0\n" \
  "v_mov_b32 v77, 0\n" \
  "v_mov_b32 v78, 0\n" \
  "v_mov_b32 v79, 0\n" \
  "v_mov_b32 v80, 0\n" \
  "v_mov_b32 v81, 0\n" \
  "v_mov_b32 v82, 0\n" \
  "v_mov_b32 v83, 0\n" \
  "v_mov_b32 v84, 0\n" \
  "v_mov_b32 v85, 0\n" \
  "v_mov_b32 v86, 0\n" \
  "v_mov_b32 v87, 0\n" \
  "s_movk_i32 s59, 880\n" \
  "s_cmp_lt_u32 s44, 34\n" \
  "s_cmovk_i32 s59, 700\n" \
  "s_cmp_ge_u32 s44, 54\n" \
  "s_cmovk_i32 s59, 520\n" \
  "s_waitcnt lgkmcnt(0)\n" \
  "s_memtime s[84:85]\n" \
  "s_add_u32 s90, s90, 1024\n" \
  "s_max_u32 s92, s91, s40\n" \
  "s_max_u32 s92, s92, s41\n" \
  "s_max_u32 s92, s92, s42\n" \
  "s_max_u32 s92, s92, s43\n" \
  "s_max_u32 s92, s92, s93\n" \
  "s_max_u32 s92, s92, s94\n" \
  "s_max_u32 s92, s92, s95\n" \
  "s_max_u32 s92, s92, s96\n" \
  "s_sub_u32 s92, s92, s91\n" \
  "s_min_u32 s92, s92, 128\n" \
  "s_mul_i32 s92, s92, 24\n" \
  "s_add_u32 s92, s92, s59\n" \
  "s_sub_u32 s58, s92, 60\n" \
  "s_waitcnt lgkmcnt(0)\n" \
  "s_add_u32 s58, s58, s82\n" \
  "s_sub_u32 s58, s58, s84\n" \
  "L_dly_%=:\n" \
  "s_sub_u32 s58, s58, 28\n" \
  "s_cmp_gt_i32 s58, 0\n" \
  "s_cbranch_scc1 L_dly_%=\n" \
  "s_waitcnt lgkmcnt(0)\n" \
  "L_ld_%=:\n" \
  "buffer_load_dwordx4 v[88:91], v6, s[36:39], s45 offen offset:0 sc1\n" \
  "s_nop 7\n" \
  "buffer_load_dwordx4 v[92:95], v6, s[36:39], s45 offen offset:2048 sc1\n" \
  "s_nop 7\n" \
  "buffer_load_dwordx4 v[96:99], v6, s[36:39], s46 offen offset:0 sc1\n" \
  "s_nop 7\n" \
  "buffer_load_dwordx4 v[100:103], v6, s[36:39], s46 offen offset:2048 sc1\n" \
  "s_waitcnt vmcnt(3)\n" \
  "v_cvt_f32_i32 v88, v88\n" \
  "v_cvt_f32_i32 v89, v89\n" \
  "v_cvt_f32_i32 v90, v90\n" \
  "v_cvt_f32_i32 v91, v91\n" \
  "v_cvt_pk_bf16_f32 v32, v88, v89\n" \
  "v_cvt_pk_bf16_f32 v33, v90, v91\n" \
  "s_waitcnt vmcnt(2)\n" \
  "v_cvt_f32_i32 v92, v92\n" \
  "v_cvt_f32_i32 v93, v93\n" \
  "v_cvt_f32_i32 v94, v94\n" \
  "v_cvt_f32_i32 v95, v95\n" \
  "v_cvt_pk_bf16_f32 v34, v92, v93\n" \
  "v_cvt_pk_bf16_f32 v35, v94, v95\n" \
  "ds_write_b128 v7, v[32:35] offset:0\n" \
  "s_waitcnt vmcnt(1)\n" \
  "v_cvt_f32_i32 v96, v96\n" \
  "v_cvt_f32_i32 v97, v97\n" \
  "v_smfmac_f32_16x16x64_bf16 v[64:67], v[32:35], a[0:7], v12\n" \
  "v_cvt_f32_i32 v98, v98\n" \
  "v_cvt_f32_i32 v99, v99\n" \
  "v_cvt_pk_bf16_f32 v36, v96, v97\n" \
  "v_cvt_pk_bf16_f32 v37, v98, v99\n" \
  "v_smfmac_f32_16x16x64_bf16 v[68:71], v[32:35], a[64:71], v12\n" \
  "s_waitcnt vmcnt(0)\n" \
  "v_cvt_f32_i32 v100, v100\n" \
  "v_cvt_f32_i32 v101, v101\n" \
  "v_cvt_f32_i32 v102, v102\n" \
  "v_cvt_f32_i32 v103, v103\n" \
  "v_smfmac_f32_16x16x64_bf16 v[72:75], v[32:35], a[128:135], v12\n" \
  "v_cvt_pk_bf16_f32 v38, v100, v101\n" \
  "v_cvt_pk_bf16_f32 v39, v102, v103\n" \
  "ds_write_b128 v7, v[36:39] offset:1024\n" \
  "v_smfmac_f32_16x16x64_bf16 v[76:79], v[32:35], a[192:199], v12\n" \
  "v_max3_f32 v23, |v88|, |v89|, |v90|\n" \
  "v_max3_f32 v23, v23, |v91|, |v92|\n" \
  "v_max3_f32 v23, v23, |v93|, |v94|\n" \
  "v_max3_f32 v23, v23, |v95|, |v96|\n" \
  "v_smfmac_f32_16x16x64_bf16 v[64:67], v[36:39], a[8:15], v12\n" \
  "v_max3_f32 v23, v23, |v97|, |v98|\n" \
  "v_max3_f32 v23, v23, |v99|, |v100|\n" \
  "v_max3_f32 v23, v23, |v101|, |v102|\n" \
  "v_max_f32 v23, v23, |v103|\n" \
  "v_cmp_lt_f32 vcc, s54, v23\n" \
  "v_smfmac_f32_16x16x64_bf16 v[68:71], v[36:39], a[72:79], v12\n" \
  "s_cbranch_vccnz L_cold_%=\n" \
  "L_cont_%=:\n" \
  "s_waitcnt lgkmcnt(0)\n" \
  "s_barrier\n" \
  "L_common_%=:\n" \
  "s_cmp_lg_u32 s69, 0\n" \
  "s_cbranch_scc1 L_late_%=\n" \
  "L_later_%=:\n" \
  "s_memrealtime s[40:41]\n" \
  "ds_read_b128 v[40:43], v8 offset:0\n" \
  "ds_read_b128 v[44:47], v8 offset:1024\n" \
  "ds_read_b128 v[48:51], v9 offset:0\n" \
  "ds_read_b128 v[52:55], v9 offset:1024\n" \
  "ds_read_b128 v[56:59], v10 offset:0\n" \
  "ds_read_b128 v[60:63], v10 offset:1024\n" \
  "v_smfmac_f32_16x16x64_bf16 v[72:75], v[36:39], a[136:143], v12\n" \
  "s_add_u32 m0, s74, s87\n" \
  "v_smfmac_f32_16x16x64_bf16 v[76:79], v[36:39], a[200:207], v12\n" \
  "buffer_load_dwordx4 v0, s[36:39], s47 offen offset:0 sc1 lds\n" \
  "v_smfmac_f32_16x16x64_bf16 v[80:83], v[32:35], v[128:135], v12\n" \
  "buffer_load_dwordx4 v0, s[36:39], s47 offen offset:1024 sc1 lds\n" \
  "v_smfmac_f32_16x16x64_bf16 v[84:87], v[32:35], v[192:199], v12\n" \
  "s_add_u32 m0, m0, 8192\n" \
  "v_smfmac_f32_16x16x64_bf16 v[80:83], v[36:39], v[136:143], v12\n" \
  "buffer_load_dwordx4 v0, s[36:39], s48 offen offset:0 sc1 lds\n" \
  "v_smfmac_f32_16x16x64_bf16 v[84:87], v[36:39], v[200:207], v12\n" \
  "buffer_load_dwordx4 v0, s[36:39], s48 offen offset:1024 sc1 lds\n" \
  "s_waitcnt lgkmcnt(5)\n" \
  "v_smfmac_f32_16x16x64_bf16 v[64:67], v[40:43], a[16:23], v12\n" \
  "v_smfmac_f32_16x16x64_bf16 v[68:71], v[40:43], a[80:87], v12\n" \
  "s_waitcnt lgkmcnt(4)\n" \
  "v_smfmac_f32_16x16x64_bf16 v[64:67], v[44:47], a[24:31], v12\n" \
  "s_and_b32 s58, %[h], 7\n" \
  "s_lshl_b32 s58, s58, 7\n" \
  "v_smfmac_f32_16x16x64_bf16 v[68:71], v[44:47], a[88:95], v12\n" \
  "s_add_u32 s58, s58, s90\n" \
  "v_mov_b32 v88, s58\n" \
  "v_smfmac_f32_16x16x64_bf16 v[72:75], v[40:43], a[144:151], v12\n" \
  "s_waitcnt lgkmcnt(0)\n" \
  "s_mov_b32 s91, s40\n" \
  "v_mov_b32 v89, s40\n" \
  "v_smfmac_f32_16x16x64_bf16 v[76:79], v[40:43], a[208:215], v12\n" \
  "s_cmp_eq_u32 s76, 3\n" \
  "s_cselect_b64 exec, 1, 0\n" \
  "global_atomic_umax v88, v89, s[36:37] offset:0\n" \
  "global_atomic_add v15, v14, s[66:67] offset:0\n" \
  "s_mov_b64 exec, -1\n" \
  "v_smfmac_f32_16x16x64_bf16 v[64:67], v[48:51], a[32:39], v12\n" \
  "v_smfmac_f32_16x16x64_bf16 v[68:71], v[48:51], a[96:103], v12\n" \
  "v_smfmac_f32_16x16x64_bf16 v[72:75], v[44:47], a[152:159], v12\n" \
  "v_smfmac_f32_16x16x64_bf16 v[76:79], v[44:47], a[216:223], v12\n" \
  "v_smfmac_f32_16x16x64_bf16 v[64:67], v[52:55], a[40:47], v12\n" \
  "v_smfmac_f32_16x16x64_bf16 v[68:71], v[52:55], a[104:111], v12\n" \
  "v_smfmac_f32_16x16x64_bf16 v[64:67], v[56:59], a[48:55], v12\n" \
  "v_smfmac_f32_16x16x64_bf16 v[68:71], v[56:59], a[112:119], v12\n" \
  "v_smfmac_f32_16x16x64_bf16 v[72:75], v[48:51], a[160:167], v12\n" \
  "v_smfmac_f32_16x16x64_bf16 v[76:79], v[48:51], a[224:231], v12\n" \
  "v_smfmac_f32_16x16x64_bf16 v[64:67], v[60:63], a[56:63], v12\n" \
  "s_cmp_eq_u32 s81, 2\n" \
  "v_smfmac_f32_16x16x64_bf16 v[68:71], v[60:63], a[120:127], v12\n" \
  "s_cbranch_scc0 L_role2_%=\n" \
  "v_smfmac_f32_16x16x64_bf16 v[72:75], v[52:55], a[168:175], v12\n" \
  "v_smfmac_f32_16x16x64_bf16 v[76:79], v[52:55], a[232:239], v12\n" \
  "s_nop 2\n" \
  "v_add_f32 v64, v64, v65\n" \
  "v_add_f32 v66, v66, v67\n" \
  "v_smfmac_f32_16x16x64_bf16 v[72:75], v[56:59], a[176:183], v12\n" \
  "v_add_f32 v68, v68, v69\n" \
  "v_add_f32 v70, v70, v71\n" \
  "v_smfmac_f32_16x16x64_bf16 v[76:79], v[56:59], a[240:247], v12\n" \
  "v_cvt_pk_bf16_f32 v24, v64, v66\n" \
  "v_cvt_pk_bf16_f32 v25, v68, v70\n" \
  "v_smfmac_f32_16x16x64_bf16 v[72:75], v[60:63], a[184:191], v12\n" \
  "ds_write2st64_b32 v4, v24, v25 offset0:32 offset1:33\n" \
  "v_smfmac_f32_16x16x64_bf16 v[76:79], v[60:63], a[248:255], v12\n" \
  "v_smfmac_f32_16x16x64_bf16 v[80:83], v[40:43], v[144:151], v12\n" \
  "v_smfmac_f32_16x16x64_bf16 v[84:87], v[40:43], v[208:215], v12\n" \
  "s_nop 3\n" \
  "v_add_f32 v72, v72, v73\n" \
  "v_add_f32 v74, v74, v75\n" \
  "v_add_f32 v76, v76, v77\n" \
  "v_smfmac_f32_16x16x64_bf16 v[80:83], v[44:47], v[152:159], v12\n" \
  "v_add_f32 v78, v78, v79\n" \
  "v_cvt_pk_bf16_f32 v24, v72, v74\n" \
  "v_cvt_pk_bf16_f32 v25, v76, v78\n" \
  "v_smfmac_f32_16x16x64_bf16 v[84:87], v[44:47], v[216:223], v12\n" \
  "ds_write2st64_b32 v5, v24, v25 offset0:40 offset1:42\n" \
  "s_waitcnt lgkmcnt(0)\n" \
  "s_barrier\n" \
  "L_x12M_%=:\n" \
  "v_smfmac_f32_16x16x64_bf16 v[80:83], v[48:51], v[160:167], v12\n" \
  "ds_read_b64_tr_b16 v[88:89], v2 offset:8192\n" \
  "ds_read_b64_tr_b16 v[90:91], v2 offset:8448\n" \
  "ds_read_b64_tr_b16 v[104:105], v3 offset:10240\n" \
  "ds_read_b64_tr_b16 v[106:107], v3 offset:10752\n" \
  "v_smfmac_f32_16x16x64_bf16 v[84:87], v[48:51], v[224:231], v12\n" \
  "ds_read_b64_tr_b16 v[92:93], v2 offset:8704\n" \
  "ds_read_b64_tr_b16 v[94:95], v2 offset:8960\n" \
  "ds_read_b64_tr_b16 v[108:109], v3 offset:11264\n" \
  "ds_read_b64_tr_b16 v[110:111], v3 offset:11776\n" \
  "v_smfmac_f32_16x16x64_bf16 v[80:83], v[52:55], v[168:175], v12\n" \
  "ds_read_b64_tr_b16 v[96:97], v2 offset:9216\n" \
  "ds_read_b64_tr_b16 v[98:99], v2 offset:9472\n" \
  "ds_read_b64_tr_b16 v[112:113], v3 offset:12288\n" \
  "ds_read_b64_tr_b16 v[114:115], v3 offset:12800\n" \
  "v_smfmac_f32_16x16x64_bf16 v[84:87], v[52:55], v[232:239], v12\n" \
  "ds_read_b64_tr_b16 v[100:101], v2 offset:9728\n" \
  "ds_read_b64_tr_b16 v[102:103], v2 offset:9984\n" \
  "ds_read_b64_tr_b16 v[116:117], v3 offset:13312\n" \
  "ds_read_b64_tr_b16 v[118:119], v3 offset:13824\n" \
  "v_smfmac_f32_16x16x64_bf16 v[80:83], v[56:59], v[176:183], v12\n" \
  "v_smfmac_f32_16x16x64_bf16 v[84:87], v[56:59], v[240:247], v12\n" \
  "s_waitcnt lgkmcnt(12)\n" \
  "v_mfma_f32_16x16x32_bf16 v[64:67], v[104:107], v[88:91], 0\n" \
  "s_waitcnt lgkmcnt(8)\n" \
  "v_mfma_f32_16x16x32_bf16 v[64:67], v[108:111], v[92:95], v[64:67]\n" \
  "s_waitcnt lgkmcnt(4)\n" \
  "v_mfma_f32_16x16x32_bf16 v[64:67], v[112:115], v[96:99], v[64:67]\n" \
  "s_waitcnt lgkmcnt(0)\n" \
  "v_mfma_f32_16x16x32_bf16 v[64:67], v[116:119], v[100:103], v[64:67]\n" \
  "v_smfmac_f32_16x16x64_bf16 v[80:83], v[60:63], v[184:191], v12\n" \
  "v_smfmac_f32_16x16x64_bf16 v[84:87], v[60:63], v[248:255], v12\n" \
  "s_nop 5\n" \
  "v_max_f32 v68, |v64|, |v65|\n" \
  "v_fma_f32 v64, v64, s53, v22\n" \
  "v_fma_f32 v65, v65, s53, v22\n" \
  "v_exp_f32 v64, v64\n" \
  "v_exp_f32 v65, v65\n" \
  "v_cmp_lt_f32 vcc, s55, v68\n" \
  "v_cvt_pk_bf16_f32 v24, v64, v65\n" \
  "ds_write_b32 v21, v24 offset:15368\n" \
  "s_cbranch_vccnz L_badM_%=\n" \
  "L_badMr_%=:\n" \
  "s_waitcnt lgkmcnt(0)\n" \
  "s_barrier\n" \
  "ds_read_b128 v[100:103], v0 offset:15360\n" \
  "ds_read_b128 v[96:99], v0 offset:17408\n" \
  "ds_read_b128 v[88:91], v20 offset:17408\n" \
  "ds_read_b128 v[92:95], v20 offset:16384\n" \
  "ds_read_b32 v76, v15 offset:14336\n" \
  "s_lshl_b32 s58, %[cq], 10\n" \
  "v_add_u32 v40, s58, v3\n" \
  "ds_read_b64_tr_b16 v[36:37], v40 offset:10240\n" \
  "ds_read_b64_tr_b16 v[38:39], v40 offset:10752\n" \
  "v_add_f32 v80, v80, v81\n" \
  "v_add_f32 v82, v82, v83\n" \
  "v_add_f32 v84, v84, v85\n" \
  "v_add_f32 v86, v86, v87\n" \
  "v_cvt_pk_bf16_f32 v32, v80, v82\n" \
  "v_cvt_pk_bf16_f32 v33, v84, v86\n" \
  "L_tail_%=:\n" \
  "s_waitcnt lgkmcnt(5)\n" \
  "v_mfma_f32_16x16x32_bf16 v[68:71], v[28:31], v[100:103], 0\n" \
  "v_mfma_f32_16x16x32_bf16 v[68:71], v[26:29], v[96:99], v[68:71]\n" \
  "v_mfma_f32_16x16x32_bf16 v[64:67], v[30:33], v[100:103], 0\n" \
  "s_waitcnt lgkmcnt(2)\n" \
  "v_mfma_f32_16x16x32_bf16 v[64:67], v[120:123], v[88:91], v[64:67]\n" \
  "v_cmp_ne_u32 vcc, 0, v76\n" \
  "v_mfma_f32_16x16x32_bf16 v[64:67], v[124:127], v[92:95], v[64:67]\n" \
  "s_cbranch_vccnz L_mx_%=\n" \
  "s_waitcnt lgkmcnt(0)\n" \
  "s_nop 0\n" \
  "v_rcp_f32 v77, v68\n" \
  "s_load_dword s40, s[36:37], s90 offset:0\n" \
  "s_load_dword s41, s[36:37], s90 offset:128\n" \
  "s_load_dword s42, s[36:37], s90 offset:256\n" \
  "s_load_dword s43, s[36:37], s90 offset:384\n" \
  "s_load_dword s93, s[36:37], s90 offset:512\n" \
  "s_load_dword s94, s[36:37], s90 offset:640\n" \
  "s_load_dword s95, s[36:37], s90 offset:768\n" \
  "s_load_dword s96, s[36:37], s90 offset:896\n" \
  "L_out_%=:\n" \
  "v_fma_f32 v72, v64, v77, s80\n" \
  "v_fma_f32 v73, v65, v77, s80\n" \
  "global_atomic_add_x2 v11, v[72:73], s[64:65] offset:0\n" \
  "v_fma_f32 v74, v66, v77, s80\n" \
  "v_fma_f32 v75, v67, v77, s80\n" \
  "global_atomic_add_x2 v11, v[74:75], s[64:65] offset:2048\n" \
  "s_memtime s[82:83]\n" \
  "buffer_store_dwordx2 v[32:33], v16, s[36:39], s50 offen sc1\n" \
  "s_mov_b64 exec, s[72:73]\n" \
  "buffer_store_dwordx4 v[36:39], v17, s[36:39], s51 offen sc1\n" \
  "s_mov_b64 exec, -1\n" \
  "s_add_u32 s50, s50, 262144\n" \
  "s_add_u32 s51, s51, 65536\n" \
  "s_lshl_b32 s58, s62, 8\n" \
  "s_add_u32 s58, s58, s79\n" \
  "s_lshl_b32 s58, s58, 8\n" \
  "s_mul_i32 s58, s58, %[lead]\n" \
  "s_add_u32 s58, s58, 4\n" \
  "v_mov_b32 v14, s58\n" \
  "s_add_u32 s66, s66, 1024\n" \
  "s_addc_u32 s67, s67, 0\n" \
  "v_and_b32 v25, 255, v13\n" \
  "v_cmp_ne_u32 vcc, 8, v25\n" \
  "s_nop 1\n" \
  "s_cmp_lg_u64 vcc, 0\n" \
  "s_cselect_b32 s69, 1, 0\n" \
  "s_cmp_lg_u32 s68, 0\n" \
  "s_cbranch_scc1 L_pend_%=\n" \
  "L_pendret_%=:\n" \
  "s_mov_b32 s68, s69\n" \
  "s_add_u32 s44, s44, 1\n" \
  "s_mov_b32 s58, s75\n" \
  "s_mov_b32 s75, s86\n" \
  "s_mov_b32 s86, s87\n" \
  "s_mov_b32 s87, s58\n" \
  "s_and_b32 s58, s76, 1\n" \
  "s_lshl_b32 s58, s58, 12\n" \
  "s_add_u32 s58, s58, s75\n" \
  "v_add_u32 v1, s58, v0\n" \
  "v_add_u32 v18, s75, v19\n" \
  "s_cmp_lt_u32 s44, 62\n" \
  "s_cselect_b32 s58, 2048, 0\n" \
  "s_cselect_b32 s59, 262144, 0\n" \
  "s_cselect_b32 s60, 1048576, 0\n" \
  "s_add_u32 s49, s49, s58\n" \
  "s_add_u32 s47, s47, s59\n" \
  "s_add_u32 s48, s48, s60\n" \
  "buffer_load_dword v13, v15, s[36:39], s49 offen offset:0 sc1\n" \
  "s_sub_u32 s58, s44, 5\n" \
  "s_cmp_ge_u32 s58, 58\n" \
  "s_cselect_b32 s69, 1, 0\n" \
  "s_mov_b32 s79, s62\n" \
  "s_sub_u32 s58, s79, 127\n" \
  "v_cvt_f32_i32 v22, s58\n" \
  "s_lshl_b32 s61, s79, 23\n" \
  "s_lshl_b32 s58, s58, 23\n" \
  "s_add_u32 s52, s58, 0x3e0293ee\n" \
  "s_add_u32 s53, s52, s58\n" \
  "s_sub_u32 s54, 0x3fba144d, s58\n" \
  "s_min_u32 s54, s54, 0x4c600000\n" \
  "s_sub_u32 s55, 0x439cd760, s58\n" \
  "s_sub_u32 s59, s55, s58\n" \
  "s_cmp_lt_u32 s76, 2\n" \
  "s_cselect_b32 s55, s55, s59\n" \
  "s_mov_b32 s62, s63\n" \
  "s_add_u32 s58, s62, 15\n" \
  "s_mul_i32 s59, s58, 0x800080\n" \
  "v_mov_b32 v26, s59\n" \
  "v_mov_b32 v27, s59\n" \
  "v_mov_b32 v28, s59\n" \
  "v_mov_b32 v29, s59\n" \
  "s_add_u32 s58, s58, 127\n" \
  "s_sub_u32 s58, s58, s79\n" \
  "s_mul_i32 s59, s58, 0x800080\n" \
  "v_mov_b32 v30, s59\n" \
  "v_mov_b32 v31, s59\n" \
  "s_add_u32 s45, s45, 32768\n" \
  "s_add_u32 s46, s45, 4096\n" \
  "s_add_u32 s64, s64, 32768\n" \
  "s_addc_u32 s65, s65, 0\n" \
  "s_mov_b32 s56, 0\n" \
  "s_lshl_b32 s58, s44, 2\n" \
  "s_add_u32 s58, s58, s76\n" \
  "s_cmp_eq_u32 s58, 7\n" \
  "s_cbranch_scc1 L_fpoll_%=\n" \
  "L_fpollr_%=:\n" \
  "s_barrier\n" \
  "s_branch L_loop_%=\n" \
  ".p2align 6\n" \
  "s_nop 0\n" \
  "s_nop 0\n" \
  "s_nop 0\n" \
  "s_nop 0\n" \
  "L_role2_%=:\n" \
  "s_cmp_lt_u32 s81, 2\n" \
  "s_cbranch_scc0 L_role3_%=\n" \
  "v_smfmac_f32_16x16x64_bf16 v[72:75], v[52:55], a[168:175], v12\n" \
  "v_smfmac_f32_16x16x64_bf16 v[76:79], v[52:55], a[232:239], v12\n" \
  "s_nop 2\n" \
  "v_add_f32 v64, v64, v65\n" \
  "v_add_f32 v66, v66, v67\n" \
  "v_smfmac_f32_16x16x64_bf16 v[72:75], v[56:59], a[176:183], v12\n" \
  "v_add_f32 v68, v68, v69\n" \
  "v_add_f32 v70, v70, v71\n" \
  "v_smfmac_f32_16x16x64_bf16 v[76:79], v[56:59], a[240:247], v12\n" \
  "v_cvt_pk_bf16_f32 v24, v64, v66\n" \
  "v_cvt_pk_bf16_f32 v25, v68, v70\n" \
  "v_smfmac_f32_16x16x64_bf16 v[72:75], v[60:63], a[184:191], v12\n" \
  "ds_write2st64_b32 v4, v24, v25 offset0:32 offset1:33\n" \
  "v_smfmac_f32_16x16x64_bf16 v[76:79], v[60:63], a[248:255], v12\n" \
  "v_smfmac_f32_16x16x64_bf16 v[80:83], v[40:43], v[144:151], v12\n" \
  "v_smfmac_f32_16x16x64_bf16 v[84:87], v[40:43], v[208:215], v12\n" \
  "s_nop 3\n" \
  "v_add_f32 v72, v72, v73\n" \
  "v_add_f32 v74, v74, v75\n" \
  "v_add_f32 v76, v76, v77\n" \
  "v_smfmac_f32_16x16x64_bf16 v[80:83], v[44:47], v[152:159], v12\n" \
  "v_add_f32 v78, v78, v79\n" \
  "v_cvt_pk_bf16_f32 v24, v72, v74\n" \
  "v_cvt_pk_bf16_f32 v25, v76, v78\n" \
  "v_smfmac_f32_16x16x64_bf16 v[84:87], v[44:47], v[216:223], v12\n" \
  "ds_write2st64_b32 v5, v24, v25 offset0:40 offset1:42\n" \
  "s_waitcnt lgkmcnt(0)\n" \
  "s_barrier\n" \
  "L_x12X_%=:\n" \
  "v_smfmac_f32_16x16x64_bf16 v[80:83], v[48:51], v[160:167], v12\n" \
  "ds_read_b64_tr_b16 v[88:89], v2 offset:8192\n" \
  "ds_read_b64_tr_b16 v[90:91], v2 offset:8448\n" \
  "ds_read_b64_tr_b16 v[92:93], v2 offset:8704\n" \
  "ds_read_b64_tr_b16 v[94:95], v2 offset:8960\n" \
  "v_smfmac_f32_16x16x64_bf16 v[84:87], v[48:51], v[224:231], v12\n" \
  "ds_read_b64_tr_b16 v[96:97], v2 offset:9216\n" \
  "ds_read_b64_tr_b16 v[98:99], v2 offset:9472\n" \
  "ds_read_b64_tr_b16 v[100:101], v2 offset:9728\n" \
  "ds_read_b64_tr_b16 v[102:103], v2 offset:9984\n" \
  "v_smfmac_f32_16x16x64_bf16 v[80:83], v[52:55], v[168:175], v12\n" \
  "v_smfmac_f32_16x16x64_bf16 v[84:87], v[52:55], v[232:239], v12\n" \
  "v_smfmac_f32_16x16x64_bf16 v[80:83], v[56:59], v[176:183], v12\n" \
  "s_waitcnt lgkmcnt(6)\n" \
  "v_mfma_f32_16x16x32_bf16 v[64:67], v[104:107], v[88:91], 0\n" \
  "s_waitcnt lgkmcnt(4)\n" \
  "v_mfma_f32_16x16x32_bf16 v[64:67], v[108:111], v[92:95], v[64:67]\n" \
  "s_waitcnt lgkmcnt(2)\n" \
  "v_mfma_f32_16x16x32_bf16 v[64:67], v[112:115], v[96:99], v[64:67]\n" \
  "s_waitcnt lgkmcnt(0)\n" \
  "v_mfma_f32_16x16x32_bf16 v[64:67], v[116:119], v[100:103], v[64:67]\n" \
  "v_smfmac_f32_16x16x64_bf16 v[84:87], v[56:59], v[240:247], v12\n" \
  "v_smfmac_f32_16x16x64_bf16 v[80:83], v[60:63], v[184:191], v12\n" \
  "s_nop 5\n" \
  "v_max3_f32 v68, |v64|, |v65|, |v66|\n" \
  "v_max_f32 v68, v68, |v67|\n" \
  "v_smfmac_f32_16x16x64_bf16 v[84:87], v[60:63], v[248:255], v12\n" \
  "v_mul_f32 v64, s52, v64\n" \
  "v_mul_f32 v65, s52, v65\n" \
  "v_mul_f32 v66, s52, v66\n" \
  "v_mul_f32 v67, s52, v67\n" \
  "v_exp_f32 v64, v64\n" \
  "v_exp_f32 v65, v65\n" \
  "v_exp_f32 v66, v66\n" \
  "v_exp_f32 v67, v67\n" \
  "v_cmp_lt_f32 vcc, s55, v68\n" \
  "v_cvt_pk_bf16_f32 v24, v64, v65\n" \
  "v_cvt_pk_bf16_f32 v25, v66, v67\n" \
  "ds_write_b64 v21, v[24:25] offset:17408\n" \
  "s_cbranch_vccnz L_badX_%=\n" \
  "L_badXr_%=:\n" \
  "s_waitcnt lgkmcnt(0)\n" \
  "s_barrier\n" \
  "ds_read_b128 v[100:103], v0 offset:15360\n" \
  "ds_read_b128 v[96:99], v0 offset:17408\n" \
  "ds_read_b128 v[88:91], v20 offset:17408\n" \
  "ds_read_b128 v[92:95], v20 offset:16384\n" \
  "ds_read_b32 v76, v15 offset:14336\n" \
  "s_lshl_b32 s58, %[cq], 10\n" \
  "v_add_u32 v40, s58, v3\n" \
  "ds_read_b64_tr_b16 v[36:37], v40 offset:10240\n" \
  "ds_read_b64_tr_b16 v[38:39], v40 offset:10752\n" \
  "v_add_f32 v80, v80, v81\n" \
  "v_add_f32 v82, v82, v83\n" \
  "v_add_f32 v84, v84, v85\n" \
  "v_add_f32 v86, v86, v87\n" \
  "v_cvt_pk_bf16_f32 v32, v80, v82\n" \
  "v_cvt_pk_bf16_f32 v33, v84, v86\n" \
  "s_branch L_tail_%=\n" \
  ".p2align 6\n" \
  "s_nop 0\n" \
  "s_nop 0\n" \
  "L_role3_%=:\n" \
  "v_smfmac_f32_16x16x64_bf16 v[72:75], v[52:55], a[168:175], v12\n" \
  "v_smfmac_f32_16x16x64_bf16 v[76:79], v[52:55], a[232:239], v12\n" \
  "s_nop 4\n" \
  "v_add_f32 v64, v64, v65\n" \
  "v_add_f32 v66, v66, v67\n" \
  "v_smfmac_f32_16x16x64_bf16 v[72:75], v[56:59], a[176:183], v12\n" \
  "v_add_f32 v68, v68, v69\n" \
  "v_add_f32 v70, v70, v71\n" \
  "v_smfmac_f32_16x16x64_bf16 v[76:79], v[56:59], a[240:247], v12\n" \
  "v_cvt_pk_bf16_f32 v24, v64, v66\n" \
  "v_cvt_pk_bf16_f32 v25, v68, v70\n" \
  "v_smfmac_f32_16x16x64_bf16 v[72:75], v[60:63], a[184:191], v12\n" \
  "ds_write2st64_b32 v4, v24, v25 offset0:32 offset1:33\n" \
  "v_smfmac_f32_16x16x64_bf16 v[76:79], v[60:63], a[248:255], v12\n" \
  "v_smfmac_f32_16x16x64_bf16 v[80:83], v[40:43], v[144:151], v12\n" \
  "v_smfmac_f32_16x16x64_bf16 v[84:87], v[40:43], v[208:215], v12\n" \
  "s_nop 3\n" \
  "v_add_f32 v72, v72, v73\n" \
  "v_add_f32 v74, v74, v75\n" \
  "v_add_f32 v76, v76, v77\n" \
  "v_smfmac_f32_16x16x64_bf16 v[80:83], v[44:47], v[152:159], v12\n" \
  "v_add_f32 v78, v78, v79\n" \
  "v_cvt_pk_bf16_f32 v24, v72, v74\n" \
  "v_cvt_pk_bf16_f32 v25, v76, v78\n" \
  "v_smfmac_f32_16x16x64_bf16 v[84:87], v[44:47], v[216:223], v12\n" \
  "ds_write2st64_b32 v5, v24, v25 offset0:40 offset1:42\n" \
  "s_waitcnt lgkmcnt(0)\n" \
  "s_barrier\n" \
  "L_x12I_%=:\n" \
  "v_smfmac_f32_16x16x64_bf16 v[80:83], v[48:51], v[160:167], v12\n" \
  "v_smfmac_f32_16x16x64_bf16 v[84:87], v[48:51], v[224:231], v12\n" \
  "v_smfmac_f32_16x16x64_bf16 v[80:83], v[52:55], v[168:175], v12\n" \
  "v_smfmac_f32_16x16x64_bf16 v[84:87], v[52:55], v[232:239], v12\n" \
  "v_smfmac_f32_16x16x64_bf16 v[80:83], v[56:59], v[176:183], v12\n" \
  "v_smfmac_f32_16x16x64_bf16 v[84:87], v[56:59], v[240:247], v12\n" \
  "v_smfmac_f32_16x16x64_bf16 v[80:83], v[60:63], v[184:191], v12\n" \
  "v_smfmac_f32_16x16x64_bf16 v[84:87], v[60:63], v[248:255], v12\n" \
  "s_nop 6\n" \
  "v_add_f32 v80, v80, v81\n" \
  "v_add_f32 v82, v82, v83\n" \
  "v_add_f32 v84, v84, v85\n" \
  "v_add_f32 v86, v86, v87\n" \
  "v_cvt_pk_bf16_f32 v32, v80, v82\n" \
  "v_cvt_pk_bf16_f32 v33, v84, v86\n" \
  "s_waitcnt lgkmcnt(0)\n" \
  "s_barrier\n" \
  "ds_read_b128 v[100:103], v0 offset:15360\n" \
  "ds_read_b128 v[96:99], v0 offset:17408\n" \
  "ds_read_b128 v[88:91], v20 offset:17408\n" \
  "ds_read_b128 v[92:95], v20 offset:16384\n" \
  "ds_read_b32 v76, v15 offset:14336\n" \
  "s_lshl_b32 s58, %[cq], 10\n" \
  "v_add_u32 v40, s58, v3\n" \
  "ds_read_b64_tr_b16 v[36:37], v40 offset:10240\n" \
  "ds_read_b64_tr_b16 v[38:39], v40 offset:10752\n" \
  "s_branch L_tail_%=\n" \
  "s_nop 7\n" \
  "s_nop 1\n" \
  "L_pend_%=:\n" \
  "s_add_u32 s60, s44, 1\n" \
  "s_cmp_lt_u32 s44, 5\n" \
  "s_cbranch_scc0 L_pend1_%=\n" \
  "s_waitcnt vmcnt(4)\n" \
  "v_and_b32 v25, 255, v23\n" \
  "v_cmp_eq_u32 vcc, 8, v25\n" \
  "s_nop 3\n" \
  "s_cbranch_vccnz L_pend2_%=\n" \
  "L_pend1_%=:\n" \
  "s_lshl_b32 s58, s60, 11\n" \
  "s_add_u32 s58, s58, s89\n" \
  "L_fws_%=:\n" \
  "buffer_load_dword v25, v15, s[36:39], s58 offen offset:0 sc1\n" \
  "s_waitcnt vmcnt(0)\n" \
  "v_and_b32 v25, 255, v25\n" \
  "v_cmp_eq_u32 vcc, 8, v25\n" \
  "s_nop 3\n" \
  "s_cbranch_vccnz L_fwds_%=\n" \
  "s_cmp_lg_u32 s57, 0\n" \
  "s_cbranch_scc1 L_fwds_%=\n" \
  "s_sleep 2\n" \
  "s_add_u32 s56, s56, 1\n" \
  "s_cmp_lt_u32 s56, 400000\n" \
  "s_cbranch_scc1 L_fws_%=\n" \
  "s_mov_b32 s57, 1\n" \
  "L_fwds_%=:\n" \
  "s_mov_b32 s56, 0\n" \
  "L_pend2_%=:\n" \
  "s_lshl_b32 s58, s60, 18\n" \
  "s_add_u32 s58, s58, s88\n" \
  "s_lshl_b32 s59, s60, 20\n" \
  "s_add_u32 s59, s59, %[oU]\n" \
  "s_add_u32 m0, s74, s86\n" \
  "s_nop 0\n" \
  "buffer_load_dwordx4 v0, s[36:39], s58 offen offset:0 sc1 lds\n" \
  "buffer_load_dwordx4 v0, s[36:39], s58 offen offset:1024 sc1 lds\n" \
  "s_add_u32 m0, m0, 8192\n" \
  "s_nop 0\n" \
  "buffer_load_dwordx4 v0, s[36:39], s59 offen offset:0 sc1 lds\n" \
  "buffer_load_dwordx4 v0, s[36:39], s59 offen offset:1024 sc1 lds\n" \
  "s_cmp_lt_u32 s60, 5\n" \
  "s_cbranch_scc1 L_pendret_%=\n" \
  "s_waitcnt vmcnt(0)\n" \
  "s_branch L_pendret_%=\n" \
  "L_late_%=:\n" \
  "s_cmp_eq_u32 s44, 63\n" \
  "s_cbranch_scc1 L_final_%=\n" \
  "ds_read_b128 v[104:107], v1 offset:0\n" \
  "ds_read_b128 v[108:111], v1 offset:1024\n" \
  "ds_read_b128 v[112:115], v1 offset:2048\n" \
  "ds_read_b128 v[116:119], v1 offset:3072\n" \
  "ds_read_b128 v[120:123], v18 offset:8192\n" \
  "ds_read_b128 v[124:127], v18 offset:9216\n" \
  "s_add_u32 s58, s44, 1\n" \
  "s_lshl_b32 s58, s58, 11\n" \
  "s_add_u32 s58, s58, s89\n" \
  "buffer_load_dword v23, v15, s[36:39], s58 offen offset:0 sc1\n" \
  "s_waitcnt lgkmcnt(0)\n" \
  "s_branch L_later_%=\n" \
  "s_nop 7\n" \
  "s_nop 1\n" \
  "L_fpoll_%=:\n" \
  "v_lshrrev_b32 v42, 7, v0\n" \
  "v_lshlrev_b32 v42, 13, v42\n" \
  "v_and_b32 v43, 127, v0\n" \
  "v_add_u32 v42, v42, v43\n" \
  "s_lshl_b32 s58, %[h], 8\n" \
  "s_add_u32 s58, s58, s45\n" \
  "s_mov_b32 exec_hi, 0\n" \
  "L_fp_%=:\n" \
  "buffer_load_dwordx4 v[44:47], v42, s[36:39], s58 offen offset:0 sc1\n" \
  "s_waitcnt vmcnt(0)\n" \
  "v_cvt_f32_i32 v44, v44\n" \
  "v_cvt_f32_i32 v45, v45\n" \
  "v_cvt_f32_i32 v46, v46\n" \
  "v_cvt_f32_i32 v47, v47\n" \
  "v_max3_f32 v44, |v44|, |v45|, |v46|\n" \
  "v_max_f32 v44, v44, |v47|\n" \
  "v_cmp_le_f32 vcc, 0x4c680000, v44\n" \
  "s_add_u32 s56, s56, 1\n" \
  "s_nop 3\n" \
  "s_cbranch_vccz L_fpd_%=\n" \
  "s_sleep 1\n" \
  "s_cmp_lt_u32 s56, 64\n" \
  "s_cbranch_scc1 L_fp_%=\n" \
  "L_fpd_%=:\n" \
  "s_mov_b64 exec, -1\n" \
  "s_mov_b32 s56, 0\n" \
  "s_branch L_fpollr_%=\n" \
  "L_first_%=:\n" \
  "s_memtime s[82:83]\n" \
  "s_waitcnt lgkmcnt(0)\n" \
  "s_sub_u32 s82, s82, 700\n" \
  "s_mov_b32 s91, -1\n" \
  "s_branch L_firstr_%=\n" \
  "s_nop 7\n" \
  "s_nop 1\n" \
  "L_cold_%=:\n" \
  "s_waitcnt lgkmcnt(0)\n" \
  "v_cmp_le_f32 vcc, 0x4c680000, v23\n" \
  "s_cmp_lg_u32 s57, 0\n" \
  "s_nop 3\n" \
  "s_cbranch_scc1 L_big_%=\n" \
  "s_cbranch_vccz L_big_%=\n" \
  "s_add_u32 s56, s56, 1\n" \
  "v_mov_b32 v64, 0\n" \
  "v_mov_b32 v65, 0\n" \
  "v_mov_b32 v66, 0\n" \
  "v_mov_b32 v67, 0\n" \
  "v_mov_b32 v68, 0\n" \
  "v_mov_b32 v69, 0\n" \
  "v_mov_b32 v70, 0\n" \
  "v_mov_b32 v71, 0\n" \
  "v_mov_b32 v72, 0\n" \
  "v_mov_b32 v73, 0\n" \
  "v_mov_b32 v74, 0\n" \
  "v_mov_b32 v75, 0\n" \
  "v_mov_b32 v76, 0\n" \
  "v_mov_b32 v77, 0\n" \
  "v_mov_b32 v78, 0\n" \
  "v_mov_b32 v79, 0\n" \
  "s_cmp_lt_u32 s56, 40000\n" \
  "s_cselect_b32 s57, 0, 1\n" \
  "s_nop 3\n" \
  "s_branch L_ld_%=\n" \
  "s_nop 7\n" \
  "s_nop 1\n" \
  "L_big_%=:\n" \
  "v_mov_b32 v24, v23\n" \
  "v_mov_b32 v25, v24\n" \
  "s_nop 1\n" \
  "v_permlane32_swap_b32 v24, v25\n" \
  "v_max_f32 v24, v24, v25\n" \
  "v_mov_b32 v25, v24\n" \
  "s_nop 1\n" \
  "v_permlane16_swap_b32 v24, v25\n" \
  "v_max_f32 v24, v24, v25\n" \
  "s_nop 1\n" \
  "v_max_f32_dpp v24, v24, v24 row_ror:8 row_mask:0xf bank_mask:0xf\n" \
  "s_nop 1\n" \
  "v_max_f32_dpp v24, v24, v24 row_ror:4 row_mask:0xf bank_mask:0xf\n" \
  "s_nop 1\n" \
  "v_max_f32_dpp v24, v24, v24 row_ror:2 row_mask:0xf bank_mask:0xf\n" \
  "s_nop 1\n" \
  "v_max_f32_dpp v24, v24, v24 row_ror:1 row_mask:0xf bank_mask:0xf\n" \
  "s_nop 3\n" \
  "s_mov_b64 exec, 1\n" \
  "ds_max_u32 v15, v24 offset:14336\n" \
  "s_mov_b64 exec, -1\n" \
  "s_waitcnt lgkmcnt(0)\n" \
  "s_branch L_cont_%=\n" \
  "s_nop 7\n" \
  "s_nop 1\n" \
  "L_badX_%=:\n" \
  "v_mov_b32 v68, 0x80000000\n" \
  "s_mov_b64 exec, 1\n" \
  "ds_or_b32 v15, v68 offset:14336\n" \
  "s_mov_b64 exec, -1\n" \
  "s_waitcnt lgkmcnt(0)\n" \
  "s_branch L_badXr_%=\n" \
  "s_nop 7\n" \
  "s_nop 1\n" \
  "L_badM_%=:\n" \
  "v_mov_b32 v68, 0x80000000\n" \
  "s_mov_b64 exec, 1\n" \
  "ds_or_b32 v15, v68 offset:14336\n" \
  "s_mov_b64 exec, -1\n" \
  "s_waitcnt lgkmcnt(0)\n" \
  "s_branch L_badMr_%=\n" \
  "s_nop 7\n" \
  "s_nop 1\n" \
  "L_mx_%=:\n" \
  "s_waitcnt lgkmcnt(0)\n" \
  "s_barrier\n" \
  "ds_write_b32 v15, v15 offset:14336\n" \
  "v_and_b32 v24, 0x7fffffff, v76\n" \
  "v_mul_f32 v24, s61, v24\n" \
  "v_mul_f32 v24, 0x41b5999a, v24\n" \
  "v_max_f32 v24, 0x42040000, v24\n" \
  "v_mul_f32 v24, s77, v24\n" \
  "v_bfe_u32 v24, v24, 23, 8\n" \
  "v_add_u32 v24, -19, v24\n" \
  "v_max_i32 v24, 72, v24\n" \
  "v_min_i32 v24, 182, v24\n" \
  "s_nop 0\n" \
  "v_readfirstlane_b32 s62, v24\n" \
  "s_nop 1\n" \
  "s_add_u32 s58, s62, 15\n" \
  "s_mul_i32 s59, s58, 0x800080\n" \
  "v_mov_b32 v26, s59\n" \
  "v_mov_b32 v27, s59\n" \
  "v_mov_b32 v28, s59\n" \
  "v_mov_b32 v29, s59\n" \
  "s_add_u32 s58, s58, 127\n" \
  "s_sub_u32 s58, s58, s79\n" \
  "s_mul_i32 s59, s58, 0x800080\n" \
  "v_mov_b32 v30, s59\n" \
  "v_mov_b32 v31, s59\n" \
  "v_add_u32 v24, s75, v0\n" \
  "ds_read_b64_tr_b16 v[88:89], v2 offset:8192\n" \
  "ds_read_b64_tr_b16 v[90:91], v2 offset:8448\n" \
  "ds_read_b64_tr_b16 v[92:93], v2 offset:8704\n" \
  "ds_read_b64_tr_b16 v[94:95], v2 offset:8960\n" \
  "ds_read_b64_tr_b16 v[96:97], v2 offset:9216\n" \
  "ds_read_b64_tr_b16 v[98:99], v2 offset:9472\n" \
  "ds_read_b64_tr_b16 v[100:101], v2 offset:9728\n" \
  "ds_read_b64_tr_b16 v[102:103], v2 offset:9984\n" \
  "s_waitcnt lgkmcnt(0)\n" \
  "ds_read_b64_tr_b16 v[48:49], v3 offset:10240\n" \
  "ds_read_b64_tr_b16 v[50:51], v3 offset:10752\n" \
  "ds_read_b64_tr_b16 v[52:53], v3 offset:11264\n" \
  "ds_read_b64_tr_b16 v[54:55], v3 offset:11776\n" \
  "ds_read_b64_tr_b16 v[56:57], v3 offset:12288\n" \
  "ds_read_b64_tr_b16 v[58:59], v3 offset:12800\n" \
  "ds_read_b64_tr_b16 v[60:61], v3 offset:13312\n" \
  "ds_read_b64_tr_b16 v[62:63], v3 offset:13824\n" \
  "s_waitcnt lgkmcnt(0)\n" \
  "ds_read_b128 v[104:107], v24 offset:0\n" \
  "ds_read_b128 v[108:111], v24 offset:1024\n" \
  "ds_read_b128 v[112:115], v24 offset:2048\n" \
  "ds_read_b128 v[116:119], v24 offset:3072\n" \
  "s_waitcnt lgkmcnt(0)\n" \
  "v_mfma_f32_16x16x32_bf16 v[64:67], v[104:107], v[88:91], 0\n" \
  "v_mfma_f32_16x16x32_bf16 v[64:67], v[108:111], v[92:95], v[64:67]\n" \
  "v_mfma_f32_16x16x32_bf16 v[64:67], v[112:115], v[96:99], v[64:67]\n" \
  "v_mfma_f32_16x16x32_bf16 v[64:67], v[116:119], v[100:103], v[64:67]\n" \
  "v_mfma_f32_16x16x32_bf16 v[72:75], v[48:51], v[88:91], 0\n" \
  "v_mfma_f32_16x16x32_bf16 v[72:75], v[52:55], v[92:95], v[72:75]\n" \
  "v_mfma_f32_16x16x32_bf16 v[72:75], v[56:59], v[96:99], v[72:75]\n" \
  "v_mfma_f32_16x16x32_bf16 v[72:75], v[60:63], v[100:103], v[72:75]\n" \
  "s_nop 7\n" \
  "ds_read_b128 v[104:107], v24 offset:4096\n" \
  "ds_read_b128 v[108:111], v24 offset:5120\n" \
  "ds_read_b128 v[112:115], v24 offset:6144\n" \
  "ds_read_b128 v[116:119], v24 offset:7168\n" \
  "s_waitcnt lgkmcnt(0)\n" \
  "v_mfma_f32_16x16x32_bf16 v[68:71], v[104:107], v[88:91], 0\n" \
  "v_mfma_f32_16x16x32_bf16 v[68:71], v[108:111], v[92:95], v[68:71]\n" \
  "v_mfma_f32_16x16x32_bf16 v[68:71], v[112:115], v[96:99], v[68:71]\n" \
  "v_mfma_f32_16x16x32_bf16 v[68:71], v[116:119], v[100:103], v[68:71]\n" \
  "v_mul_f32 v64, s52, v64\n" \
  "v_mul_f32 v65, s52, v65\n" \
  "v_mul_f32 v66, s52, v66\n" \
  "v_mul_f32 v67, s52, v67\n" \
  "s_nop 3\n" \
  "v_mul_f32 v68, s52, v68\n" \
  "v_mul_f32 v69, s52, v69\n" \
  "v_mul_f32 v70, s52, v70\n" \
  "v_mul_f32 v71, s52, v71\n" \
  "v_mul_f32 v72, s53, v72\n" \
  "v_mul_f32 v73, s53, v73\n" \
  "v_max3_f32 v78, v64, v65, v66\n" \
  "v_max3_f32 v78, v78, v67, v68\n" \
  "v_max3_f32 v78, v78, v69, v70\n" \
  "v_max3_f32 v78, v78, v71, v72\n" \
  "v_max_f32 v78, v78, v73\n" \
  "v_mov_b32 v79, v78\n" \
  "s_nop 1\n" \
  "v_permlane16_swap_b32 v78, v79\n" \
  "v_max_f32 v78, v78, v79\n" \
  "v_mov_b32 v79, v78\n" \
  "s_nop 1\n" \
  "v_permlane32_swap_b32 v78, v79\n" \
  "v_max_f32 v78, v78, v79\n" \
  "v_sub_f32 v79, v22, v78\n" \
  "v_sub_f32 v64, v64, v78\n" \
  "v_sub_f32 v65, v65, v78\n" \
  "v_sub_f32 v66, v66, v78\n" \
  "v_sub_f32 v67, v67, v78\n" \
  "v_sub_f32 v68, v68, v78\n" \
  "v_sub_f32 v69, v69, v78\n" \
  "v_sub_f32 v70, v70, v78\n" \
  "v_sub_f32 v71, v71, v78\n" \
  "v_add_f32 v72, v72, v79\n" \
  "v_add_f32 v73, v73, v79\n" \
  "v_exp_f32 v64, v64\n" \
  "v_exp_f32 v65, v65\n" \
  "v_exp_f32 v66, v66\n" \
  "v_exp_f32 v67, v67\n" \
  "v_exp_f32 v68, v68\n" \
  "v_exp_f32 v69, v69\n" \
  "v_exp_f32 v70, v70\n" \
  "v_exp_f32 v71, v71\n" \
  "v_exp_f32 v72, v72\n" \
  "v_exp_f32 v73, v73\n" \
  "v_cvt_pk_bf16_f32 v104, v64, v65\n" \
  "v_cvt_pk_bf16_f32 v105, v66, v67\n" \
  "v_cvt_pk_bf16_f32 v106, v68, v69\n" \
  "v_cvt_pk_bf16_f32 v107, v70, v71\n" \
  "v_cvt_pk_bf16_f32 v79, v72, v73\n" \
  "v_cndmask_b32 v116, v79, v15, s[70:71]\n" \
  "v_cndmask_b32 v117, v15, v79, s[70:71]\n" \
  "s_nop 1\n" \
  "v_mfma_f32_16x16x16_bf16 v[68:71], v[30:31], v[116:117], 0\n" \
  "v_mfma_f32_16x16x16_bf16 v[64:67], v[32:33], v[116:117], 0\n" \
  "v_mfma_f32_16x16x32_bf16 v[108:111], v[120:123], v[104:107], 0\n" \
  "v_mfma_f32_16x16x32_bf16 v[112:115], v[124:127], v[104:107], 0\n" \
  "s_nop 3\n" \
  "v_mfma_f32_16x16x32_bf16 v[68:71], v[26:29], v[104:107], v[68:71]\n" \
  "s_nop 7\n" \
  "v_cndmask_b32 v108, v108, v112, s[70:71]\n" \
  "v_cndmask_b32 v109, v109, v113, s[70:71]\n" \
  "v_cndmask_b32 v110, v110, v114, s[70:71]\n" \
  "v_cndmask_b32 v111, v111, v115, s[70:71]\n" \
  "v_add_f32 v64, v64, v108\n" \
  "v_add_f32 v65, v65, v109\n" \
  "v_add_f32 v66, v66, v110\n" \
  "v_add_f32 v67, v67, v111\n" \
  "v_rcp_f32 v77, v68\n" \
  "s_load_dword s40, s[36:37], s90 offset:0\n" \
  "s_load_dword s41, s[36:37], s90 offset:128\n" \
  "s_load_dword s42, s[36:37], s90 offset:256\n" \
  "s_load_dword s43, s[36:37], s90 offset:384\n" \
  "s_load_dword s93, s[36:37], s90 offset:512\n" \
  "s_load_dword s94, s[36:37], s90 offset:640\n" \
  "s_load_dword s95, s[36:37], s90 offset:768\n" \
  "s_load_dword s96, s[36:37], s90 offset:896\n" \
  "s_nop 3\n" \
  "s_branch L_out_%=\n" \
  "L_final_%=:\n" \
  "ds_read_b128 v[40:43], v8 offset:0\n" \
  "ds_read_b128 v[44:47], v8 offset:1024\n" \
  "ds_read_b128 v[48:51], v9 offset:0\n" \
  "ds_read_b128 v[52:55], v9 offset:1024\n" \
  "ds_read_b128 v[56:59], v10 offset:0\n" \
  "ds_read_b128 v[60:63], v10 offset:1024\n" \
  "v_smfmac_f32_16x16x64_bf16 v[72:75], v[36:39], a[136:143], v12\n" \
  "s_cmp_eq_u32 s76, 3\n" \
  "v_smfmac_f32_16x16x64_bf16 v[76:79], v[36:39], a[200:207], v12\n" \
  "s_cselect_b64 exec, 1, 0\n" \
  "global_atomic_add v15, v14, s[66:67] offset:0\n" \
  "s_mov_b64 exec, -1\n" \
  "v_smfmac_f32_16x16x64_bf16 v[80:83], v[32:35], v[128:135], v12\n" \
  "s_lshl_b32 s58, s76, 12\n" \
  "v_smfmac_f32_16x16x64_bf16 v[84:87], v[32:35], v[192:199], v12\n" \
  "s_add_u32 s58, s58, %[oWarm]\n" \
  "v_smfmac_f32_16x16x64_bf16 v[80:83], v[36:39], v[136:143], v12\n" \
  "s_add_u32 s58, s58, 5242880\n" \
  "v_smfmac_f32_16x16x64_bf16 v[84:87], v[36:39], v[200:207], v12\n" \
  "buffer_load_dwordx4 v[104:107], v0, s[36:39], s58 offen offset:0 sc1\n" \
  "s_waitcnt lgkmcnt(5)\n" \
  "v_smfmac_f32_16x16x64_bf16 v[72:75], v[40:43], a[144:151], v12\n" \
  "buffer_load_dwordx4 v[108:111], v0, s[36:39], s58 offen offset:1024 sc1\n" \
  "v_smfmac_f32_16x16x64_bf16 v[76:79], v[40:43], a[208:215], v12\n" \
  "s_waitcnt lgkmcnt(4)\n" \
  "v_smfmac_f32_16x16x64_bf16 v[72:75], v[44:47], a[152:159], v12\n" \
  "v_smfmac_f32_16x16x64_bf16 v[76:79], v[44:47], a[216:223], v12\n" \
  "s_waitcnt lgkmcnt(3)\n" \
  "v_smfmac_f32_16x16x64_bf16 v[72:75], v[48:51], a[160:167], v12\n" \
  "v_smfmac_f32_16x16x64_bf16 v[76:79], v[48:51], a[224:231], v12\n" \
  "s_waitcnt lgkmcnt(2)\n" \
  "v_smfmac_f32_16x16x64_bf16 v[72:75], v[52:55], a[168:175], v12\n" \
  "v_smfmac_f32_16x16x64_bf16 v[76:79], v[52:55], a[232:239], v12\n" \
  "s_waitcnt lgkmcnt(1)\n" \
  "v_smfmac_f32_16x16x64_bf16 v[72:75], v[56:59], a[176:183], v12\n" \
  "v_smfmac_f32_16x16x64_bf16 v[76:79], v[56:59], a[240:247], v12\n" \
  "s_waitcnt lgkmcnt(0)\n" \
  "v_smfmac_f32_16x16x64_bf16 v[72:75], v[60:63], a[184:191], v12\n" \
  "v_smfmac_f32_16x16x64_bf16 v[76:79], v[60:63], a[248:255], v12\n" \
  "v_smfmac_f32_16x16x64_bf16 v[80:83], v[40:43], v[144:151], v12\n" \
  "v_smfmac_f32_16x16x64_bf16 v[84:87], v[40:43], v[208:215], v12\n" \
  "s_nop 4\n" \
  "v_add_f32 v72, v72, v73\n" \
  "v_add_f32 v74, v74, v75\n" \
  "v_smfmac_f32_16x16x64_bf16 v[80:83], v[44:47], v[152:159], v12\n" \
  "v_add_f32 v76, v76, v77\n" \
  "v_add_f32 v78, v78, v79\n" \
  "v_smfmac_f32_16x16x64_bf16 v[84:87], v[44:47], v[216:223], v12\n" \
  "v_cvt_pk_bf16_f32 v24, v72, v74\n" \
  "v_cvt_pk_bf16_f32 v25, v76, v78\n" \
  "v_smfmac_f32_16x16x64_bf16 v[80:83], v[48:51], v[160:167], v12\n" \
  "ds_write2st64_b32 v5, v24, v25 offset0:40 offset1:42\n" \
  "v_smfmac_f32_16x16x64_bf16 v[84:87], v[48:51], v[224:231], v12\n" \
  "s_waitcnt lgkmcnt(0)\n" \
  "s_barrier\n" \
  "ds_write_b32 v15, v15 offset:14336\n" \
  "s_lshl_b32 s58, %[cq], 10\n" \
  "v_add_u32 v40, s58, v3\n" \
  "ds_read_b64_tr_b16 v[36:37], v40 offset:10240\n" \
  "ds_read_b64_tr_b16 v[38:39], v40 offset:10752\n" \
  "v_smfmac_f32_16x16x64_bf16 v[80:83], v[52:55], v[168:175], v12\n" \
  "v_smfmac_f32_16x16x64_bf16 v[84:87], v[52:55], v[232:239], v12\n" \
  "v_smfmac_f32_16x16x64_bf16 v[80:83], v[56:59], v[176:183], v12\n" \
  "v_smfmac_f32_16x16x64_bf16 v[84:87], v[56:59], v[240:247], v12\n" \
  "v_smfmac_f32_16x16x64_bf16 v[80:83], v[60:63], v[184:191], v12\n" \
  "v_smfmac_f32_16x16x64_bf16 v[84:87], v[60:63], v[248:255], v12\n" \
  "s_nop 7\n" \
  "v_add_f32 v80, v80, v81\n" \
  "v_add_f32 v82, v82, v83\n" \
  "v_add_f32 v84, v84, v85\n" \
  "v_add_f32 v86, v86, v87\n" \
  "v_cvt_pk_bf16_f32 v32, v80, v82\n" \
  "v_cvt_pk_bf16_f32 v33, v84, v86\n" \
  "buffer_store_dwordx2 v[32:33], v16, s[36:39], s50 offen sc1\n" \
  "s_mov_b64 exec, s[72:73]\n" \
  "s_waitcnt lgkmcnt(0)\n" \
  "buffer_store_dwordx4 v[36:39], v17, s[36:39], s51 offen sc1\n" \
  "s_mov_b64 exec, -1\n" \
  "s_add_u32 s50, s50, 262144\n" \
  "s_add_u32 s51, s51, 65536\n" \
  "s_add_u32 s78, s78, 1\n" \
  "s_cmp_lt_u32 s78, %[l1]\n" \
  "s_cbranch_scc1 L_layer_%=\n" \
  "s_waitcnt vmcnt(2)\n" \
  "s_waitcnt lgkmcnt(0)\n" \
  "s_barrier\n" \
  "v_mov_b32 v0, v104\n" \
  "v_mov_b32 v1, v105\n" \
  "v_mov_b32 v2, v106\n" \
  "v_mov_b32 v3, v107\n" \
  "v_mov_b32 v4, v108\n" \
  "v_mov_b32 v5, v109\n" \
  "v_mov_b32 v6, v110\n" \
  "v_mov_b32 v7, v111\n" \

#define REC_ASM_CLOBBERS "v8", "v9", "v10", "v11", "v12", "v13", "v14", "v15", "v16", "v17", "v18", "v19", "v20", "v21", "v22", "v23", "v24", "v25", "v26", "v27", "v28", "v29", "v30", "v31", "v32", "v33", "v34", "v35", "v36", "v37", "v38", "v39", "v40", "v41", "v42", "v43", "v44", "v45", "v46", "v47", "v48", "v49", "v50", "v51", "v52", "v53", "v54", "v55", "v56", "v57", "v58", "v59", "v60", "v61", "v62", "v63", "v64", "v65", "v66", "v67", "v68", "v69", "v70", "v71", "v72", "v73", "v74", "v75", "v76", "v77", "v78", "v79", "v80", "v81", "v82", "v83", "v84", "v85", "v86", "v87", "v88", "v89", "v90", "v91", "v92", "v93", "v94", "v95", "v96", "v97", "v98", "v99", "v100", "v101", "v102", "v103", "v104", "v105", "v106", "v107", "v108", "v109", "v110", "v111", "v112", "v113", "v114", "v115", "v116", "v117", "v118", "v119", "v120", "v121", "v122", "v123", "v124", "v125", "v126", "v127", "v128", "v129", "v130", "v131", "v132", "v133", "v134", "v135", "v136", "v137", "v138", "v139", "v140", "v141", "v142", "v143", "v144", "v145", "v146", "v147", "v148", "v149", "v150", "v151", "v152", "v153", "v154", "v155", "v156", "v157", "v158", "v159", "v160", "v161", "v162", "v163", "v164", "v165", "v166", "v167", "v168", "v169", "v170", "v171", "v172", "v173", "v174", "v175", "v176", "v177", "v178", "v179", "v180", "v181", "v182", "v183", "v184", "v185", "v186", "v187", "v188", "v189", "v190", "v191", "v192", "v193", "v194", "v195", "v196", "v197", "v198", "v199", "v200", "v201", "v202", "v203", "v204", "v205", "v206", "v207", "v208", "v209", "v210", "v211", "v212", "v213", "v214", "v215", "v216", "v217", "v218", "v219", "v220", "v221", "v222", "v223", "v224", "v225", "v226", "v227", "v228", "v229", "v230", "v231", "v232", "v233", "v234", "v235", "v236", "v237", "v238", "v239", "v240", "v241", "v242", "v243", "v244", "v245", "v246", "v247", "v248", "v249", "v250", "v251", "v252", "v253", "v254", "v255", "a0", "a1", "a2", "a3", "a4", "a5", "a6", "a7", "a8", "a9", "a10", "a11", "a12", "a13", "a14", "a15", "a16", "a17", "a18", "a19", "a20", "a21", "a22", "a23", "a24", "a25", "a26", "a27", "a28", "a29", "a30", "a31", "a32", "a33", "a34", "a35", "a36", "a37", "a38", "a39", "a40", "a41", "a42", "a43", "a44", "a45", "a46", "a47", "a48", "a49", "a50", "a51", "a52", "a53", "a54", "a55", "a56", "a57", "a58", "a59", "a60", "a61", "a62", "a63", "a64", "a65", "a66", "a67", "a68", "a69", "a70", "a71", "a72", "a73", "a74", "a75", "a76", "a77", "a78", "a79", "a80", "a81", "a82", "a83", "a84", "a85", "a86", "a87", "a88", "a89", "a90", "a91", "a92", "a93", "a94", "a95", "a96", "a97", "a98", "a99", "a100", "a101", "a102", "a103", "a104", "a105", "a106", "a107", "a108", "a109", "a110", "a111", "a112", "a113", "a114", "a115", "a116", "a117", "a118", "a119", "a120", "a121", "a122", "a123", "a124", "a125", "a126", "a127", "a128", "a129", "a130", "a131", "a132", "a133", "a134", "a135", "a136", "a137", "a138", "a139", "a140", "a141", "a142", "a143", "a144", "a145", "a146", "a147", "a148", "a149", "a150", "a151", "a152", "a153", "a154", "a155", "a156", "a157", "a158", "a159", "a160", "a161", "a162", "a163", "a164", "a165", "a166", "a167", "a168", "a169", "a170", "a171", "a172", "a173", "a174", "a175", "a176", "a177", "a178", "a179", "a180", "a181", "a182", "a183", "a184", "a185", "a186", "a187", "a188", "a189", "a190", "a191", "a192", "a193", "a194", "a195", "a196", "a197", "a198", "a199", "a200", "a201", "a202", "a203", "a204", "a205", "a206", "a207", "a208", "a209", "a210", "a211", "a212", "a213", "a214", "a215", "a216", "a217", "a218", "a219", "a220", "a221", "a222", "a223", "a224", "a225", "a226", "a227", "a228", "a229", "a230", "a231", "a232", "a233", "a234", "a235", "a236", "a237", "a238", "a239", "a240", "a241", "a242", "a243", "a244", "a245", "a246", "a247", "a248", "a249", "a250", "a251", "a252", "a253", "a254", "a255", "s36", "s37", "s38", "s39", "s40", "s41", "s42", "s43", "s44", "s45", "s46", "s47", "s48", "s49", "s50", "s51", "s52", "s53", "s54", "s55", "s56", "s57", "s58", "s59", "s60", "s61", "s62", "s63", "s64", "s65", "s66", "s67", "s68", "s69", "s70", "s71", "s72", "s73", "s74", "s75", "s76", "s77", "s78", "s79", "s80", "s81", "s82", "s83", "s84", "s85", "s86", "s87", "s88", "s89", "s90", "s91", "s92", "s93", "s94", "s95", "s96", "vcc", "scc", "memory"

typedef __amdgpu_buffer_rsrc_t rsrc_t;
constexpr int SC1 = 16;
#define DRAIN() asm volatile("s_waitcnt vmcnt(0)" ::: "memory")
#define WAITV(n) asm volatile("s_waitcnt vmcnt(%0)" ::"n"(n) : "memory")
#define BAR() do { asm volatile("s_waitcnt lgkmcnt(0)" ::: "memory"); __builtin_amdgcn_s_barrier(); asm volatile("" ::: "memory"); } while (0)
#define LGKM() asm volatile("s_waitcnt lgkmcnt(0)" ::: "memory")
#define SCHED() __builtin_amdgcn_sched_barrier(0)
__device__ __forceinline__ v4u ld(rsrc_t rs, unsigned voff, unsigned soff) { return __builtin_amdgcn_raw_buffer_load_b128(rs, voff, soff, 0); }
__device__ __forceinline__ v4u lds1(rsrc_t rs, unsigned voff, unsigned soff) { return __builtin_amdgcn_raw_buffer_load_b128(rs, voff, soff, SC1); }
#define EXP_ST SC1
__device__ __forceinline__ void sts1(v4u v, rsrc_t rs, unsigned voff, unsigned soff) { __builtin_amdgcn_raw_buffer_store_b128(v, rs, voff, soff, EXP_ST); }
__device__ __forceinline__ unsigned ldflag(const unsigned char* ws, unsigned off) { return __hip_atomic_load((gu32*)(ws + off), RLX_AGENT); }
__device__ __forceinline__ void addflag(unsigned char* ws, unsigned off, unsigned v) { (void)__hip_atomic_fetch_add((gu32*)(ws + off), v, RLX_AGENT); }
__device__ __forceinline__ void setflag(unsigned char* ws, unsigned off, unsigned v) { __hip_atomic_store((gu32*)(ws + off), v, RLX_AGENT); }
constexpr unsigned OFF_TMO = OFF_CTL + 32;
template <int SL = 6>
__device__ __forceinline__ bool spin_fail(unsigned& n, unsigned char* ws) {
  __builtin_amdgcn_s_sleep(SL);
  asm volatile("" ::: "memory");
  ++n;
  if ((n & 255u) == 0u) {
    if (n > 200000u) __hip_atomic_store((gu32*)(ws + OFF_TMO), 1u, RLX_AGENT);
    if (__builtin_amdgcn_readfirstlane(ldflag(ws, OFF_TMO))) return true;
  }
  return false;
}
constexpr unsigned FAST = 60;
#define FA 62u
constexpr unsigned NFA = (64 - FA) * 16;
#define NREP 1
constexpr unsigned NF0 = 512 + 64 + 64 + 136 + 8 * NREP;
constexpr unsigned NFILL = NF0 + 10 * NFA + 32;
__device__ __forceinline__ unsigned fill_off(unsigned u) {
  if (u >= NFILL - 32) return OFF_XNA + 2 * MB + (u - (NFILL - 32)) * 4096;
  if (u >= NF0) {
    u -= NF0;
    const unsigned lo = u < 5 * NFA ? KY_L0 : 0u;
    if (u >= 5 * NFA) u -= 5 * NFA;
    return (u < NFA ? OFF_KM + FA * 65536 + u * 4096 : OFF_YF + FA * 262144 + (u - NFA) * 4096) - lo;
  }
  if (u < 512) return OFF_PART + ((u >> 8) * 64 + FAST) * 262144 + (u & 255) * 4096;
  if (u < 576) return OFF_XNB + ((u >> 5) & 1) * 2 * MB + FAST * 32768 + (u & 31) * 4096;
  if (u < 640) return OFF_PQ + ((u >> 5) & 1) * 2 * MB + FAST * 32768 + (u & 31) * 4096;
  return OFF_KV0 + (u - 640) * 4096;
}
__device__ __forceinline__ unsigned umax4(unsigned m, v4u v) { return max(max(m, max(v.x, v.y)), max(v.z, v.w)); }
__device__ __forceinline__ bool hole(unsigned m) { return __builtin_amdgcn_ballot_w64(m == 0xFFFFFFFFu) != 0ull; }
#define ORDER() __builtin_amdgcn_fence(__ATOMIC_ACQUIRE, "wavefront")

#define TLOG(tag)
#define TLOG2(tag)
struct Ctx {
  unsigned char* ws;
  rsrc_t rs;
  v4u srd;
  LAS unsigned char* L;
  int lane, w, fr, g;
  mutable unsigned tln;
};

__device__ __forceinline__ void dma_tile(const Ctx& c, const unsigned (&vo)[8], unsigned goff, unsigned loff) {
  asm volatile(
      "s_mov_b32 m0, %[l0]\n s_nop 0\n"
      "buffer_load_dwordx4 %[v0], %[rs], %[s0] offen sc1 lds\n"
      "buffer_load_dwordx4 %[v1], %[rs], %[s0] offen offset:1024 sc1 lds\n"
      "buffer_load_dwordx4 %[v2], %[rs], %[s0] offen offset:2048 sc1 lds\n"
      "buffer_load_dwordx4 %[v3], %[rs], %[s0] offen offset:3072 sc1 lds\n"
      "s_mov_b32 m0, %[l1]\n s_nop 0\n"
      "buffer_load_dwordx4 %[v4], %[rs], %[s1] offen sc1 lds\n"
      "buffer_load_dwordx4 %[v5], %[rs], %[s1] offen offset:1024 sc1 lds\n"
      "buffer_load_dwordx4 %[v6], %[rs], %[s1] offen offset:2048 sc1 lds\n"
      "buffer_load_dwordx4 %[v7], %[rs], %[s1] offen offset:3072 sc1 lds\n"
      :
      : [l0] "s"(loff), [l1] "s"(loff + 4096), [s0] "s"(goff), [s1] "s"(goff + 4096), [rs] "s"(c.srd), [v0] "v"(vo[0]), [v1] "v"(vo[1]), [v2] "v"(vo[2]),
        [v3] "v"(vo[3]), [v4] "v"(vo[4]), [v5] "v"(vo[5]), [v6] "v"(vo[6]), [v7] "v"(vo[7])
      : "memory");
}

struct Pf { unsigned go, lo; u64 em; };
template <int R>
__device__ __forceinline__ void dma_row(const Ctx& c, unsigned vo, const Pf& p) {
  asm volatile(
      "s_mov_b64 exec, %[em]\n s_add_u32 m0, %[l], %[lo]\n s_nop 0\n"
      "buffer_load_dwordx4 %[v], %[rs], %[s] offen offset:%[o] sc1 lds\n"
      "s_mov_b64 exec, -1\n"
      :
      : [em] "s"(p.em), [l] "s"(p.lo), [lo] "n"((R >> 2) * 4096), [s] "s"(p.go + (R >> 2) * 4096), [rs] "s"(c.srd), [v] "v"(vo), [o] "n"((R & 3) * 1024)
      : "scc");
}
#define PD 2
#define FENCE() __builtin_amdgcn_sched_barrier(0)
constexpr int RING = 4;
#define EARLY 12
#define SL_ST 12
#define SL_P 6
#define SL_D 2
#define SL_E 4
#define SL_E0 20
#define GATE_E 63
#define SCH_Q 0x2490u
#define SCH_K 0x4925u
#define SCH_V 0x924Au
#define TK_SL 40
#define CPF_ST 64
#define TLBN 1
#define TLBS (2u << 20)
#define CPF_HI (1u << 30)
#define CPF_I0 8
#define CPF_I1 4
#define WB_N 2
#define LNF 0
#define EPP 1
#define S2N 44u
#define SL_R 6
template <int NST, bool REFRESH, int POLL, class Body, class Pub, class Tail, class Hook>
__device__ __forceinline__ void stream(const Ctx& c, unsigned xoff, bool ep, unsigned flagoff, unsigned want, int s0, int stride, int n, Body body, Pub pub, Tail tail, Hook hook) {
  constexpr int NS = (RING - 1) * NST + (RING - 2) * 8;
  unsigned vo[8];
#pragma unroll
  for (int r = 0; r < 8; ++r) vo[r] = (unsigned)((c.lane ^ (8 * (c.w & 1) + r)) * 16);
  u64 mask = 0ull;
  int next = 0;
  int pubd = 0;
  int streak = 0;
  const unsigned wrow = (unsigned)c.w * 8192;
  LAS v2u* lm;
  auto ready = [&](int it) { return ((mask >> (s0 + it * stride)) & 1ull) != 0; };
  auto issue = [&](int it) { dma_tile(c, vo, xoff + (unsigned)(s0 + it * stride) * 32768 + wrow, (unsigned)(it & (RING - 1)) * 32768 + wrow); };
  auto pubto = [&](int done) { for (; pubd <= done; ++pubd) if (c.w == 0) pub(s0 + pubd * stride); };
  unsigned em = 0;
  bool unver = false;
  auto getmask = [&]() {
    const v2u m = *lm; mask = ((u64)(unsigned)__builtin_amdgcn_readfirstlane(m.y) << 32) | (unsigned)__builtin_amdgcn_readfirstlane(m.x);
    if (POLL == 2) em = __builtin_amdgcn_readfirstlane(lm[8].x);
  };
  auto poll = [&]() {
    const unsigned f = ldflag(c.ws, flagoff + c.lane * 4);
    mask = __builtin_amdgcn_ballot_w64(f >= want);
    if (POLL == 2) em = ep ? (unsigned)__builtin_amdgcn_ballot_w64((f & 0x10001u) == 0x10001u) : 0u;
  };
  auto put = [&]() { *lm = (v2u){(unsigned)mask, (unsigned)(mask >> 32)}; if (POLL == 2) lm[8].x = em; };
  auto verify = [&](int it) {
    unsigned sp = 0;
    for (;;) {
      unsigned m = 0;
#pragma unroll
      for (int r = 0; r < 8; ++r) m = umax4(m, *(const LAS v4u*)(c.L + (unsigned)(it & (RING - 1)) * 32768 + wrow + r * 1024 + c.lane * 16));
      if (!hole(m) || spin_fail<SL_D>(sp, c.ws)) break;
      issue(it);
      DRAIN();
    }
  };
  for (int i = 0; i < n; ++i) {
    lm = (LAS v2u*)(c.L + RING * 32768 + 8192 + (i & 1) * 8);
    if (streak >= RING - 1) {
      if (i < EARLY) { WAITV(NST + 8); BAR(); pubto(i - 2); }
      else { WAITV(NS); BAR(); pubto(i - RING); }
    } else if (next <= i) {
      DRAIN();
      BAR();
      pubto(i - 1);
      const int gb = s0 + i * stride - (POLL == 1 && i == n - 1 ? 2 : 0);
      if (!((mask >> gb) & 1ull)) {
        if (c.w == 0) {
          unsigned sp = 0;
          for (;;) {
            poll();
            if ((mask >> gb) & 1ull) break;
            if (POLL == 2 && i < 2 && ((em >> gb) & 1u)) break;
            if (spin_fail<SL_ST>(sp, c.ws)) { mask = ~0ull; break; }
          }
          put();
        }
        BAR();
        getmask();
      }
      const bool sure = ready(i);
      issue(i);
      next = i + 1;
      if (next < n && ready(next)) { issue(next); ++next; }
      const bool rf = REFRESH && next < n && !ready(i + RING - 1 < n ? i + RING - 1 : n - 1);
      lm += 2;
      if (rf && c.w == 0) { poll(); put(); }
      if (next > i + 1) WAITV(8); else DRAIN();
      if (POLL && !sure) verify(i);
      BAR();
      if (rf) getmask();
      if (POLL == 2 && next == 1 && !ready(1) && ((em >> (s0 + stride)) & 1u)) { issue(1); next = 2; unver = true; }
    } else {
      const bool rf = REFRESH && next < n && !ready(i + RING - 1 < n ? i + RING - 1 : n - 1);
      if (rf && c.w == 0) { poll(); put(); }
      DRAIN();
      if (POLL == 2 && unver) { verify(i); unver = false; }
      BAR();
      if (rf) getmask();
      pubto(i - 1);
    }
    Pf pf = {xoff + (unsigned)(s0 + next * stride) * 32768 + wrow, (unsigned)(next & (RING - 1)) * 32768 + wrow, 0ull};
    if (next == i + RING - 1 && next < n && ready(next)) { pf.em = ~0ull; ++next; ++streak; }
    else {
      streak = 0;
      while (next < i + RING && next < n && ready(next)) { issue(next); ++next; }
      if (next == i + RING) streak = 1;
    }
    auto mid = [&]() { if (pubd < i) { if (pf.em) WAITV(8); else DRAIN(); } BAR(); pubto(i - 1); };
    body(s0 + i * stride, (unsigned)(i & (RING - 1)) * 32768, pf, vo, mid);
    if (i < 2) { DRAIN(); BAR(); pubto(i); TLOG(6); }
    hook(i);
  }
  tail();
  DRAIN();
  BAR();
  pubto(n - 1);
}
template <int I> struct IC { static constexpr int value = I; };
template <int N, int I = 0, class F>
__device__ __forceinline__ void static_for(F& f) { if constexpr (I < N) { f(IC<I>{}); static_for<N, I + 1>(f); } }
#define LANDED(x) asm volatile("" ::"v"(x))
#define SGB(m, n) __builtin_amdgcn_sched_group_barrier(m, n, 0)
#define SGB1(m, n) __builtin_amdgcn_sched_group_barrier(m, n, 1)

__device__ __forceinline__ void xfrag_addr(const Ctx& c, unsigned (&xa)[4]) {
#pragma unroll
  for (int q = 0; q < 4; ++q) xa[q] = (unsigned)(c.fr * 1024 + (((q ^ (c.fr >> 2)) << 2) + (c.g ^ (c.fr & 3))) * 16);
}
__device__ __forceinline__ v4u xfrag(const Ctx& c, const unsigned (&xa)[4], unsigned base, int t, int ks) {
  return *(const LAS v4u*)(c.L + base + xa[ks & 3] + t * 16384 + (ks >> 2) * 256);
}

constexpr unsigned OOB = 0x80000000u;
template <int N>
__device__ __forceinline__ void ln_rows(const v4u (&xi)[N], v4u (&o)[N]) {
  float x[N][8], s[N], q[N];
#pragma unroll
  for (int i = 0; i < N; ++i) {
#pragma unroll
    for (int j = 0; j < 4; ++j) { x[i][2 * j] = lo16(xi[i][j]); x[i][2 * j + 1] = hi16(xi[i][j]); }
    s[i] = ((x[i][0] + x[i][1]) + (x[i][2] + x[i][3])) + ((x[i][4] + x[i][5]) + (x[i][6] + x[i][7]));
  }
#pragma unroll
  for (int i = 0; i < N; ++i) s[i] = wave_sum(s[i]) * (1.f / DM);
#pragma unroll
  for (int i = 0; i < N; ++i) {
    q[i] = 0.f;
#pragma unroll
    for (int j = 0; j < 8; ++j) { float d = x[i][j] - s[i]; q[i] += d * d; }
  }
#pragma unroll
  for (int i = 0; i < N; ++i) q[i] = wave_sum(q[i]);
#pragma unroll
  for (int i = 0; i < N; ++i) {
    const float var = rbf(q[i] * (1.f / DM)), mb = rbf(s[i]);
    const float r = __builtin_amdgcn_rcpf(rbf(__builtin_amdgcn_sqrtf(rbf(var + 1e-5f))));
#pragma unroll
    for (int j = 0; j < 4; ++j) { const unsigned d = pack2(x[i][2 * j] - mb, x[i][2 * j + 1] - mb); o[i][j] = pack2(lo16(d) * r, hi16(d) * r); }
  }
}
__device__ __forceinline__ v4u addbf(v4u a, v4u b) {
  return (v4u){pack2(lo16(a.x) + lo16(b.x), hi16(a.x) + hi16(b.x)), pack2(lo16(a.y) + lo16(b.y), hi16(a.y) + hi16(b.y)),
               pack2(lo16(a.z) + lo16(b.z), hi16(a.z) + hi16(b.z)), pack2(lo16(a.w) + lo16(b.w), hi16(a.w) + hi16(b.w))};
}
typedef short v4s __attribute__((ext_vector_type(4)));
__device__ __forceinline__ v4u mksrd(const void* p, unsigned bytes) { const u64 a = (u64)p; return (v4u){(unsigned)a, (unsigned)(a >> 32), bytes, 0x00020000u}; }
__device__ __forceinline__ void dma1(v4u srd, unsigned voff, unsigned soff, unsigned loff) {
  asm volatile("s_mov_b32 m0, %[l]\n s_nop 0\n buffer_load_dwordx4 %[v], %[rs], %[s] offen lds\n" ::[l] "s"(loff), [s] "s"(soff), [rs] "s"(srd), [v] "v"(voff) : "memory");
}
__device__ __forceinline__ void code_warm(const Ctx& c, unsigned lo, unsigned hi) {
  u64 pc; int d; unsigned len;
  asm volatile("s_getpc_b64 %0\nL_cpf%=:\n s_mov_b32 %1, L_cpf%=-L_cbeg\n s_mov_b32 %2, L_ce1-L_cbeg\n s_max_i32 %2, %2, 0" : "=s"(pc), "=s"(d), "=s"(len) : : "scc");
  const rsrc_t rc = __builtin_amdgcn_make_buffer_rsrc((void*)(pc - (long long)d), 0, len, 0x00020000);
  unsigned v[4];
#pragma unroll
  for (int k = 0; k < 4; ++k) { const unsigned o = (unsigned)(k * 4 + c.w) * 8192; v[k] = __builtin_amdgcn_raw_buffer_load_b32(rc, (o >= lo && o < hi) ? (unsigned)c.lane * 128 : OOB, o, 0); }
  DRAIN();
#pragma unroll
  for (int k = 0; k < 4; ++k) asm volatile("" ::"v"(v[k]));
}
__device__ __forceinline__ v4u trd2(const Ctx& c, unsigned o0, unsigned o1) {
  const v2u a = __builtin_bit_cast(v2u, __builtin_amdgcn_ds_read_tr16_b64_v4i16((LAS v4s*)(c.L + o0)));
  const v2u b = __builtin_bit_cast(v2u, __builtin_amdgcn_ds_read_tr16_b64_v4i16((LAS v4s*)(c.L + o1)));
  return (v4u){a.x, a.y, b.x, b.y};
}
template <bool VU>
__device__ __forceinline__ void role_s1(const Ctx& c, int l, int mat, int h, int par, const u16* Wm, const u16* Wom, const u16* xin) {
  const unsigned fl = OFF_FLG + l * FL_SIZE;
  const int q = c.fr >> 2, p = c.fr & 3;
  const int hp = 4 * (h & 7) + (h >> 3);
  v4u W[2][16];
  {
    const v4u srd = mksrd(Wm, DM * HD * 2);
    const unsigned v0 = (unsigned)(c.g * 8192 + ((c.fr ^ (2 * c.g)) * 16)), v1 = (unsigned)(c.g * 8192 + ((c.fr ^ (2 * c.g + 8)) * 16));
    const unsigned j0 = (blockIdx.x - 8) & 127, r0 = (j0 * 16 + 4 * c.w) * 1024, l16 = (unsigned)c.lane * 16;
    const unsigned hv = c.lane < 8 ? (unsigned)c.lane * 1024 : OOB, hb = OFF_HS + (blockIdx.x & 3) * 256;
    unsigned hr = 0;
    auto tick = [&](int k) { asm volatile("s_mov_b32 m0, %[l]\n s_nop 0\n buffer_load_dword %[v], %[rs], %[s] offen sc1 lds\n" ::[l] "s"(147456 + 256 * k), [s] "s"(hb), [rs] "s"(c.srd), [v] "v"(hv) : "memory"); };
    auto same = [&](int k) { return __builtin_amdgcn_ballot_w64(*(const volatile LAS unsigned*)(c.L + 147456 + 256 * k + c.lane * 4) == hr) & 255ull; };
    if (l == 0) {
      if (c.w == 0) tick(0);
      const v4u sx = mksrd(xin, S_TOK * DM * 2);
#pragma unroll
      for (int i = 0; i < 4; ++i) dma1(sx, l16, r0 + i * 1024, 131072 + c.w * 4096 + i * 1024);
    }
    auto ld_w = [&]() {
#pragma unroll 4
      for (int j = 0; j < 32; ++j) dma1(srd, (j & 2) ? v1 : v0, (unsigned)(hp * 256 + (32 * c.w + j) * 32768), (unsigned)(32 * c.w + j) * 1024);
    };
    const bool lnf = false;
    if (!lnf) ld_w();
    if (l == 0) {
      if (c.w == 0) { __builtin_amdgcn_s_sleep(TK_SL); tick(1); if (lnf) WAITV(1); else WAITV(33); } else
      if (lnf) DRAIN(); else WAITV(32);
      if (c.w == 0) hr = *(const LAS unsigned*)(c.L + 147456 + c.lane * 4);
      v4u x0[4], xn[4];
#pragma unroll
      for (int i = 0; i < 4; ++i) x0[i] = *(const LAS v4u*)(c.L + 131072 + c.w * 4096 + i * 1024 + l16);
      ln_rows<4>(x0, xn);
#pragma unroll
      for (int i = 0; i < 4; ++i) sts1(xn[i], c.rs, l16 + i * 1024, OFF_XNA + r0);
      if (c.w == 0) {
        TLOG2(8);
        tick(2);
        WAITV(1);
        u64 m = same(1);
        for (unsigned sp = 0; m && sp < 400000u; ++sp) { DRAIN(); m &= same(2); if (m) tick(2); }
        TLOG2(9);
        if (j0 >= 120 && c.lane < 8) addflag(c.ws, OFF_CTL + c.lane * 4096 + 48, 1u);
      }
    }
    if (l != 0 || c.w != 0)
    DRAIN();
    BAR();
    if (l == 0 && c.w == 0) {
      __builtin_amdgcn_raw_buffer_store_b16((short)3, c.rs, c.lane < 32 ? (unsigned)c.lane * 256 : OOB, OFF_FLG + FL_X + j0 * 2, SC1);
      if (j0 < 120 && c.lane < 8) addflag(c.ws, OFF_CTL + c.lane * 4096 + 48, 1u);
    }
    if (lnf) { ld_w(); DRAIN(); BAR(); }
#pragma unroll
    for (int a = 0; a < 2; ++a) {
      const unsigned b = (unsigned)(c.g * 2048 + q * 256 + (((4 * c.w + 2 * a + (p >> 1)) ^ (2 * (q + 4 * (c.g & 1)))) * 16) + (p & 1) * 8);
#pragma unroll
      for (int ks = 0; ks < 16; ++ks) W[a][ks] = trd2(c, b + ks * 8192, b + ks * 8192 + 1024);
    }
    BAR();
  }
  v4u Wo[VU ? 8 : 1][4];
  if (VU) {
    const v4u srd = mksrd(Wom, DM * HD * 2);
#pragma unroll 8
    for (int j = 0; j < 32; ++j) dma1(srd, (unsigned)((c.lane ^ (2 * (j & 7))) * 16), (unsigned)(hp * 128 + 32 * c.w + j) * 1024, (unsigned)(32 * c.w + j) * 1024);
    DRAIN();
    BAR();
#pragma unroll
    for (int ct = 0; ct < 8; ++ct) {
      const unsigned b = (unsigned)((4 * c.g + q) * 1024 + (((16 * c.w + 2 * ct + (p >> 1)) ^ (2 * (q + 4 * (c.g & 1)))) * 16) + (p & 1) * 8);
#pragma unroll
      for (int ks = 0; ks < 4; ++ks) Wo[ct][ks] = trd2(c, b + ks * 32768, b + ks * 32768 + 16384);
    }
    BAR();
  }
  unsigned xa[4];
  xfrag_addr(c, xa);
#pragma unroll
  for (int a = 0; a < 2; ++a)
#pragma unroll
    for (int ks = 0; ks < 16; ++ks) LANDED(W[a][ks]);
  if (VU) {
#pragma unroll
    for (int ct = 0; ct < 8; ++ct)
#pragma unroll
      for (int ks = 0; ks < 4; ++ks) LANDED(Wo[ct][ks]);
  }
  TLOG(5);
  const unsigned obase = VU ? OFF_UF + (unsigned)(h * 32 + 8 * c.w) * 1024 : (mat == 0 ? OFF_QF : OFF_KF + l * 16 * MB) + (unsigned)(h * 8 + c.w) * 1024;
  const unsigned l16 = (unsigned)c.lane * 16;
  auto body = [&](int seg, unsigned base, const Pf& pf, const unsigned (&vo)[8], auto& mid) {
    f32x4 acc[2][2] = {};
    v4u xb[PD + 1][2];
#pragma unroll
    for (int k = 0; k < PD; ++k) { xb[k][0] = xfrag(c, xa, base, 0, k); xb[k][1] = xfrag(c, xa, base, 1, k); }
    FENCE();
    auto step = [&](auto KS) {
      constexpr int ks = decltype(KS)::value;
      const v4u x0 = xb[ks % (PD + 1)][0], x1 = xb[ks % (PD + 1)][1];
      acc[0][0] = mfma16(W[0][ks], x0, acc[0][0]);
      if (ks + PD < 16) xb[(ks + PD) % (PD + 1)][0] = xfrag(c, xa, base, 0, ks + PD);
      FENCE();
      acc[1][0] = mfma16(W[1][ks], x0, acc[1][0]);
      if (ks + PD < 16) xb[(ks + PD) % (PD + 1)][1] = xfrag(c, xa, base, 1, ks + PD);
      FENCE();
      acc[0][1] = mfma16(W[0][ks], x1, acc[0][1]);
      FENCE();
      if ((ks & 1) == 0) dma_row<ks / 2>(c, vo[ks / 2], pf);
      FENCE();
      acc[1][1] = mfma16(W[1][ks], x1, acc[1][1]);
      FENCE();
    };
    static_for<16>(step);
    v4u f0 = pack8(acc[0][0], acc[1][0]), f1 = pack8(acc[0][1], acc[1][1]);
    if (!VU) {
      sts1(f0, c.rs, l16, obase + (unsigned)seg * 262144);
      sts1(f1, c.rs, l16, obase + (unsigned)seg * 262144 + 4096);
    } else {
      LAS unsigned char* vb = c.L + RING * 32768;
      *(LAS v4u*)(vb + c.w * 1024 + l16) = f0;
      *(LAS v4u*)(vb + 4096 + c.w * 1024 + l16) = f1;
      mid();
      v4u V[2][4];
#pragma unroll
      for (int ks = 0; ks < 4; ++ks)
#pragma unroll
        for (int t = 0; t < 2; ++t) V[t][ks] = *(const LAS v4u*)(vb + t * 4096 + ks * 1024 + l16);
      SGB1(0x100, 8);
      f32x4 u[4][4] = {};
#pragma unroll
      for (int cp = 0; cp < 5; ++cp) {
        if (cp < 4) {
#pragma unroll
          for (int ks = 0; ks < 4; ++ks) {
            u[cp][0] = mfma16(V[0][ks], Wo[2 * cp][ks], u[cp][0]); u[cp][1] = mfma16(V[1][ks], Wo[2 * cp][ks], u[cp][1]);
            u[cp][2] = mfma16(V[0][ks], Wo[2 * cp + 1][ks], u[cp][2]); u[cp][3] = mfma16(V[1][ks], Wo[2 * cp + 1][ks], u[cp][3]);
          }
        }
        if (cp > 0) {
          const int cq = cp - 1;
          sts1(pack8(u[cq][0], u[cq][1]), c.rs, l16 + (cq & 1) * 2048, obase + (unsigned)seg * 1048576 + (cq >> 1) * 4096);
          sts1(pack8(u[cq][2], u[cq][3]), c.rs, l16 + (cq & 1) * 2048 + 1024, obase + (unsigned)seg * 1048576 + (cq >> 1) * 4096);
        }
        if (cp == 0) SGB1(0x008, 16);
        else if (cp < 4) {
          SGB1(0x008, 2);
#pragma unroll
          for (int j = 0; j < 6; ++j) { SGB1(0x008, 1); SGB1(0x002, 2); }
          SGB1(0x040, 1);
#pragma unroll
          for (int j = 0; j < 6; ++j) { SGB1(0x008, 1); SGB1(0x002, 2); }
          SGB1(0x040, 1);
          SGB1(0x008, 2);
        }
      }
    }
  };
  const unsigned addv = mat == 0 ? 1024u : 4u;
  auto pub = [&](int seg) { if (c.lane == 0) addflag(c.ws, fl + FL_QKV + (unsigned)(seg * 32 + h) * 64, addv); };
  auto tail = []() {};
  auto hook = [&](int i) {
    if (i == 3 && l == 0 && mat != 0) {
      unsigned f = ~0u;
      asm volatile("" : "+v"(f));
      for (unsigned u = (blockIdx.x - 160) * 4 + c.w; u < NFILL; u += 384) {
        const unsigned o = fill_off(u);
#pragma unroll
        for (int k = 0; k < 4; ++k) sts1((v4u){f, f, f, f}, c.rs, l16 + k * 1024, o);
      }
    }
  };
  const unsigned rep = (blockIdx.x & 31) * 256;
  if (VU) stream<8, true, EPX>(c, OFF_XNA + l * 2 * MB, l == 1, fl + FL_X + rep, 0x30003u, par, 2, 32, body, pub, tail, hook);
  else stream<2, true, EPX>(c, OFF_XNA + l * 2 * MB, l == 1 && mat != 0, fl + FL_X + rep, 0x30003u, 0, 1, 64, body, pub, tail, hook);
}

__device__ __forceinline__ float phi(float t) { return rbf((t > 0.f ? rbf(t + 1.f) : rbf(__expf(t))) + 1e-6f); }
__device__ __forceinline__ f32x4 phi4(f32x4 a) { return (f32x4){phi(rbf(a[0])), phi(rbf(a[1])), phi(rbf(a[2])), phi(rbf(a[3]))}; }
__device__ __forceinline__ void role_s3b(const Ctx& c, int l, int cls, int par, const u16* Bq) {
  const unsigned fl = OFF_FLG + l * FL_SIZE;
  const int hb = 4 * cls + c.w;
  v4u W[3][16];
  {
    const v4u srd = mksrd(Bq, DM * 3 * DM * 2);
    const unsigned vo = (unsigned)((c.lane >> 1) * 3072 + (c.lane & 1) * 16), lb = (unsigned)c.w * 32768;
    const unsigned ra = lb + (unsigned)((8 * c.g + (c.fr >> 2)) * 32 + (c.fr & 3) * 8);
    auto stage = [&](int a, int slot) {
#pragma unroll 4
      for (int ks = 0; ks < 16; ++ks) dma1(srd, vo, (unsigned)(ks * 98304 + a * 1024 + hb * 32), lb + slot * 16384 + ks * 1024);
    };
    stage(0, 0);
    stage(1, 1);
#pragma unroll
    for (int a = 0; a < 3; ++a) {
      if (a < 2) WAITV(16); else DRAIN();
#pragma unroll
      for (int ks = 0; ks < 16; ++ks) W[a][ks] = trd2(c, ra + (a & 1) * 16384 + ks * 1024, ra + (a & 1) * 16384 + ks * 1024 + 128);
      if (a == 0) { LGKM(); stage(2, 0); }
    }
    BAR();
  }
  unsigned xa[4];
  xfrag_addr(c, xa);
#pragma unroll
  for (int a = 0; a < 3; ++a)
#pragma unroll
    for (int ks = 0; ks < 16; ++ks) LANDED(W[a][ks]);
  const unsigned l16 = (unsigned)c.lane * 16;
  f32x4 kv = {}, zz = {};
  unsigned one = 0x3F803F80u;
  asm volatile("" : "+v"(one));
  const v4u ones = {one, one, one, one};
  const unsigned k0 = OFF_KV0 + (unsigned)((l * 32 + hb) * 3) * 2048;
  const unsigned ab = (unsigned)c.w * 8192;
  auto ld_ab = [&]() {
#pragma unroll
    for (int i = 0; i < 6; ++i)
      asm volatile("s_mov_b32 m0, %[l]\n s_nop 0\n buffer_load_dwordx4 %[v], %[rs], %[s] offen sc1 lds\n" ::[l] "s"(ab + i * 1024), [s] "s"(k0 + i * 1024), [rs] "s"(c.srd), [v] "v"(l16) : "memory");
  };
  auto body = [&](int seg, unsigned base, const Pf& pf, const unsigned (&vo)[8], auto& mid) {
    f32x4 aq[2] = {}, ak[2] = {}, av[2] = {};
    v4u xb[3][2];
    xb[0][0] = xfrag(c, xa, base, 0, 0); xb[0][1] = xfrag(c, xa, base, 1, 0);
    xb[1][0] = xfrag(c, xa, base, 0, 1); xb[1][1] = xfrag(c, xa, base, 1, 1);
    FENCE();
    auto step = [&](auto KS) {
      constexpr int ks = decltype(KS)::value;
      const v4u x0 = xb[ks % 3][0], x1 = xb[ks % 3][1];
      ak[0] = mfma16(x0, W[1][ks], ak[0]);
      if (ks + 2 < 16) xb[(ks + 2) % 3][0] = xfrag(c, xa, base, 0, ks + 2);
      FENCE();
      av[0] = mfma16(x0, W[2][ks], av[0]);
      if (ks + 2 < 16) xb[(ks + 2) % 3][1] = xfrag(c, xa, base, 1, ks + 2);
      FENCE();
      aq[0] = mfma16(W[0][ks], x0, aq[0]);
      ak[1] = mfma16(x1, W[1][ks], ak[1]);
      FENCE();
      if ((ks & 1) == 0) dma_row<ks / 2>(c, vo[ks / 2], pf);
      FENCE();
      av[1] = mfma16(x1, W[2][ks], av[1]);
      aq[1] = mfma16(W[0][ks], x1, aq[1]);
      FENCE();
    };
    static_for<16>(step);
    TLOG(0x700 + seg);
    v4u pk = pack8(phi4(ak[0]), phi4(ak[1])), vf = pack8(av[0], av[1]);
    kv = mfma16(pk, vf, kv);
    zz = mfma16(pk, ones, zz);
    sts1(pack8(phi4(aq[0]), phi4(aq[1])), c.rs, l16, OFF_PQ + (unsigned)((l * 64 + seg) * 32 + hb) * 1024);
  };
  auto pub = [&](int seg) { if (c.lane < 8) addflag(c.ws, fl + FL_PQ + c.lane * 256 + (unsigned)seg * 4, 1u); };
  auto tail = [&]() {
    if (par < 3) {
      sts1(__builtin_bit_cast(v4u, kv), c.rs, l16, k0 + par * 2048);
      sts1(__builtin_bit_cast(v4u, zz), c.rs, l16, k0 + par * 2048 + 1024);
    } else {
      v4u a[3], b[3];
      unsigned sp = 0;
      ld_ab(); DRAIN();
      for (;;) {
        unsigned m = 0;
#pragma unroll
        for (int i = 0; i < 3; ++i) { a[i] = *(const LAS v4u*)(c.L + ab + i * 2048 + l16); b[i] = *(const LAS v4u*)(c.L + ab + i * 2048 + 1024 + l16); }
#pragma unroll
        for (int i = 0; i < 3; ++i) m = umax4(umax4(m, a[i]), b[i]);
        if (!hole(m) || spin_fail<SL_D>(sp, c.ws)) break;
        ld_ab();
        DRAIN();
      }
      auto F = [](v4u x) { return __builtin_bit_cast(f32x4, x); };
      const v4u r = pack8((F(a[0]) + F(a[1])) + (F(a[2]) + kv), (F(b[0]) + F(b[1])) + (F(b[2]) + zz));
      sts1(r, c.rs, l16, OFF_KVF + (unsigned)(l * 32 + hb) * 1024);
#pragma unroll
      for (int i = 1; i < NREP; ++i) sts1(r, c.rs, l ? l16 : OOB, OFF_KVF + (unsigned)((1 + i) * 32 + hb) * 1024);
    }
  };
  auto hook = [](int) {};
  stream<1, true, 1>(c, OFF_XNB + l * 2 * MB, false, fl + FL_B + cls * 256, 8u, par, 4, 16, body, pub, tail, hook);
}

__device__ __forceinline__ void role_s2(const Ctx& c, int l, int quad, int idx, rsrc_t rx, unsigned xoff) {
  const unsigned fl = OFF_FLG + l * FL_SIZE;
  const int h = quad + 8 * c.w;
  const unsigned l16 = (unsigned)c.lane * 16;
  const float scale = 0.08838834764831845f;
  LAS unsigned* fx = (LAS unsigned*)(c.L + 16400);
  LAS unsigned char* PL = c.L;
  LAS unsigned char* PM = c.L + 8192;
  auto qkv_ok = [&](unsigned s) { return __all(ldflag(c.ws, fl + FL_QKV + (unsigned)((s & 63u) * 32 + quad + 8 * (c.lane & 3)) * 64) == 0x408u) != 0; };
  v4u q[2][4], k[2][4], u[4][8];
  auto ld_qk = [&](unsigned s, unsigned vo) {
    const unsigned qo = OFF_QF + ((s & 63u) * 32 + h) * 8192, ko = OFF_KF + l * 16 * MB + ((s & 63u) * 32 + h) * 8192;
#pragma unroll
    for (int t = 0; t < 2; ++t)
#pragma unroll
      for (int ks = 0; ks < 4; ++ks) { q[t][ks] = lds1(c.rs, vo + ks * 1024, qo + t * 4096); k[t][ks] = lds1(c.rs, vo + ks * 1024, ko + t * 4096); }
  };
  auto ld_u = [&](unsigned s, unsigned vo, int c0 = 0, int c1 = 8) {
#pragma unroll
    for (int ct = c0; ct < c1; ++ct)
#pragma unroll
      for (int hh = 0; hh < 4; ++hh) u[hh][ct] = lds1(c.rs, vo + (ct & 3) * 1024, OFF_UF + (((s & 63u) * 32 + quad + 8 * hh) * 32 + 8 * c.w + (ct & 4)) * 1024);
  };
  const int grp = idx >> 2;
  unsigned rounds = l == 0 ? (grp == 0 ? 0x2492u : grp == 1 ? 0x9249u : 0x4924u) : (grp == 0 ? SCH_Q : grp == 1 ? SCH_K : SCH_V);
  auto nexts = [&]() { const unsigned r = (unsigned)__builtin_ctz(rounds | 0x10000u); rounds &= rounds - 1u; return 4u * r + (unsigned)(idx & 3); };
  unsigned s = nexts();
  bool have = false;
  for (int k = 0; k < (idx & 3); ++k) __builtin_amdgcn_s_sleep(S2ST);
  while (s < 64u) {
    TLOG(0x300 + s);
    if (!have) {
      unsigned sp = 0;
      while (!qkv_ok(s)) if (spin_fail(sp, c.ws)) break;
      ORDER();
      ld_qk(s, l16);
      ld_u(s, l16);
    }
    SCHED();
    f32x4 s00 = {}, s01 = {}, s11 = {};
#pragma unroll
    for (int ks = 0; ks < 4; ++ks) { s00 = mfma16(k[0][ks], q[0][ks], s00); s01 = mfma16(k[0][ks], q[1][ks], s01); s11 = mfma16(k[1][ks], q[1][ks], s11); }
    float mx[2], lx[2];
#pragma unroll
    for (int j = 0; j < 4; ++j) {
      const bool ok = 4 * c.g + j <= c.fr;
      s00[j] = ok ? rbf(rbf(s00[j]) * scale) : -INFINITY;
      s01[j] = rbf(rbf(s01[j]) * scale);
      s11[j] = ok ? rbf(rbf(s11[j]) * scale) : -INFINITY;
    }
    mx[0] = x32_max(x16_max(fmaxf(fmaxf(s00[0], s00[1]), fmaxf(s00[2], s00[3]))));
    mx[1] = x32_max(x16_max(fmaxf(fmaxf(fmaxf(s01[0], s01[1]), fmaxf(s01[2], s01[3])), fmaxf(fmaxf(s11[0], s11[1]), fmaxf(s11[2], s11[3])))));
#pragma unroll
    for (int j = 0; j < 4; ++j) { s00[j] = __expf(s00[j] - mx[0]); s01[j] = __expf(s01[j] - mx[1]); s11[j] = __expf(s11[j] - mx[1]); }
    lx[0] = x32_add(x16_add((s00[0] + s00[1]) + (s00[2] + s00[3])));
    lx[1] = x32_add(x16_add(((s01[0] + s01[1]) + (s01[2] + s01[3])) + ((s11[0] + s11[1]) + (s11[2] + s11[3]))));
    LANDED(lx[1]);
    TLOG2(0x800);
    if (c.w == 0) {
      unsigned sp = 0, fv;
      const unsigned fo = fl + FL_R + ((s - (s >= FA ? 1u : 0u) - ((c.lane & 1) && s >= 2u ? 2u : 0u)) * 8 + quad) * 128;
      for (;;) {
        const unsigned f = ldflag(c.ws, fo);
        fv = __builtin_amdgcn_readfirstlane(f);
        if ((fv & 255u) == 64u) break;
        if ((__builtin_amdgcn_readlane(f, 1) & 255u) != 64u) __builtin_amdgcn_s_sleep(32);
        if (spin_fail<SL_R>(sp, c.ws)) break;
      }
      fx[0] = fv;
    }
    BAR();
    const unsigned fv = __builtin_amdgcn_readfirstlane(fx[0]), sn = nexts();
    TLOG(0x500 + s);
    const float delta = __uint_as_float(((fv >> (s >= FA ? 16 : 8)) & 255u) << 23);
    const unsigned kyo = l == 0 ? KY_L0 : 0u;
    v4u km[4], y[2][4];
    for (unsigned sp = 0;;) {
#pragma unroll
      for (int ks = 0; ks < 4; ++ks) km[ks] = lds1(c.rs, (unsigned)(c.g * 128 + (c.fr & 7) * 16 + ks * 512), OFF_KM - kyo + (s * 32 + h) * 2048);
#pragma unroll
      for (int cp = 0; cp < 4; ++cp)
#pragma unroll
        for (int p = 0; p < 2; ++p)
          y[p][cp] = lds1(c.rs, (unsigned)((c.g >> 1) * 65536 + (c.lane & 31) * 16 + cp * 512), OFF_YF - kyo + ((s * 32 + quad + 16 * p) * 16 + 4 * c.w) * 512);
      if (s < FA) break;
      unsigned m = 0;
#pragma unroll
      for (int i = 0; i < 4; ++i) m = umax4(umax4(umax4(m, km[i]), y[0][i]), y[1][i]);
      if (!hole(m) || spin_fail<SL_D>(sp, c.ws)) break;
    }
    const unsigned row = s * 32 + 4 * quad + c.w;
    const v4u xr = __builtin_amdgcn_raw_buffer_load_b128(rx, l16, xoff + row * 1024, SC1);
    SCHED();
#pragma unroll
    for (int t = 0; t < 2; ++t) {
      f32x4 sm = {};
#pragma unroll
      for (int ks = 0; ks < 4; ++ks) sm = mfma16(km[ks], q[t][ks], sm);
#pragma unroll
      for (int j = 0; j < 4; ++j) sm[j] = rbf(rbf(sm[j] * delta) * scale);
      const float m = fmaxf(mx[t], x16_max(fmaxf(fmaxf(sm[0], sm[1]), fmaxf(sm[2], sm[3]))));
      const float al = __expf(mx[t] - m);
#pragma unroll
      for (int j = 0; j < 4; ++j) sm[j] = __expf(sm[j] - m);
      const float lsum = al * lx[t] + x16_add((sm[0] + sm[1]) + (sm[2] + sm[3]));
      const float inv = 1.f / lsum, ai = al * inv, id = inv * delta;
      v4u px = t == 0 ? pack8(s00 * ai, (f32x4){0.f, 0.f, 0.f, 0.f}) : pack8(s01 * ai, s11 * ai);
      *(LAS v4u*)(PL + (c.w * 2 + t) * 1024 + l16) = px;
      if (c.g < 2) *(LAS v2u*)(PM + t * 1024 + ((2 * (c.w & 1) + c.g) * 16 + c.fr) * 16 + (c.w >> 1) * 8) = pack4(sm * id);
    }
    BAR();
    TLOG2(0x801);
    const unsigned po = OFF_PART + (l * 64 + s) * 262144;
    {
      v4u p[4][2];
#pragma unroll
      for (int hh = 0; hh < 4; ++hh) { p[hh][0] = *(const LAS v4u*)(PL + (hh * 2) * 1024 + l16); p[hh][1] = *(const LAS v4u*)(PL + (hh * 2 + 1) * 1024 + l16); }
      const v4u m0 = *(const LAS v4u*)(PM + l16), m1 = *(const LAS v4u*)(PM + 1024 + l16);
      const unsigned so = (unsigned)(c.fr * 1024 + (((c.g & 1) * 2 + (c.g >> 1)) * 16));
#pragma unroll
      for (int cp = 0; cp < 4; ++cp) {
        f32x4 a[2][2] = {};
#pragma unroll
        for (int hh = 0; hh < 4; ++hh)
#pragma unroll
          for (int e = 0; e < 2; ++e) { a[e][0] = mfma16(u[hh][2 * cp + e], p[hh][0], a[e][0]); a[e][1] = mfma16(u[hh][2 * cp + e], p[hh][1], a[e][1]); }
#pragma unroll
        for (int pp = 0; pp < 2; ++pp) {
          v2u b0 = pp ? (v2u){m0.z, m0.w} : (v2u){m0.x, m0.y}, b1 = pp ? (v2u){m1.z, m1.w} : (v2u){m1.x, m1.y};
          v2u a0 = {y[pp][cp].x, y[pp][cp].z}, a1 = {y[pp][cp].y, y[pp][cp].w};
          a[0][0] = mfma16k(a0, b0, a[0][0]); a[0][1] = mfma16k(a0, b1, a[0][1]);
          a[1][0] = mfma16k(a1, b0, a[1][0]); a[1][1] = mfma16k(a1, b1, a[1][1]);
        }
#pragma unroll
        for (int t = 0; t < 2; ++t) {
          const v2u e0 = pack4(a[0][t]), e1 = pack4(a[1][t]);
          const v2u r0 = __builtin_amdgcn_permlane16_swap(e0.x, e1.x, false, false), r1 = __builtin_amdgcn_permlane16_swap(e0.y, e1.y, false, false);
          sts1((v4u){r0.x, r1.x, r0.y, r1.y}, c.rs, so + cp * 64, po + (unsigned)(quad * 32768 + t * 16384 + c.w * 256));
        }
      }
    }
    TLOG2(0x802);
    v4u pa[8];
    auto ld_pa = [&]() {
#pragma unroll
      for (int qd = 0; qd < 8; ++qd) pa[qd] = lds1(c.rs, l16, po + (unsigned)(qd * 32768 + (4 * quad + c.w) * 1024));
    };
    have = false;
    if (s < FAST) {
      have = sn < 64u && qkv_ok(sn);
      DRAIN();
      BAR();
      TLOG2(0x803);
      if (c.w == 0) {
        if (c.lane == 0) addflag(c.ws, fl + FL_P + s * 128, 1u);
        unsigned sp = 0;
        while (__builtin_amdgcn_readfirstlane(ldflag(c.ws, fl + FL_P + s * 128)) < 8u) if (spin_fail<SL_P>(sp, c.ws)) break;
      }
      BAR();
      TLOG2(0x804);
      ld_pa();
    } else {
      unsigned sp = 0;
      for (;;) {
        ld_pa();
        unsigned m = 0;
#pragma unroll
        for (int qd = 0; qd < 8; ++qd) m = umax4(m, pa[qd]);
        if (!hole(m) || spin_fail<SL_D>(sp, c.ws)) break;
      }
    }
    const unsigned vn = have ? l16 : OOB;
    ld_qk(sn, vn);
    SCHED();
    float o[8];
#pragma unroll
    for (int j = 0; j < 4; ++j) {
      o[2 * j] = ((lo16(pa[0][j]) + lo16(pa[1][j])) + (lo16(pa[2][j]) + lo16(pa[3][j]))) + ((lo16(pa[4][j]) + lo16(pa[5][j])) + (lo16(pa[6][j]) + lo16(pa[7][j])));
      o[2 * j + 1] = ((hi16(pa[0][j]) + hi16(pa[1][j])) + (hi16(pa[2][j]) + hi16(pa[3][j]))) + ((hi16(pa[4][j]) + hi16(pa[5][j])) + (hi16(pa[6][j]) + hi16(pa[7][j])));
    }
    LANDED(o[7]);
    TLOG2(0x805);
    const v4u ob = {pack2(o[0], o[1]), pack2(o[2], o[3]), pack2(o[4], o[5]), pack2(o[6], o[7])};
    v4u x1 = {pack2(lo16(xr.x) + lo16(ob.x), hi16(xr.x) + hi16(ob.x)), pack2(lo16(xr.y) + lo16(ob.y), hi16(xr.y) + hi16(ob.y)),
              pack2(lo16(xr.z) + lo16(ob.z), hi16(xr.z) + hi16(ob.z)), pack2(lo16(xr.w) + lo16(ob.w), hi16(xr.w) + hi16(ob.w))};
    float x[8] = {lo16(x1.x), hi16(x1.x), lo16(x1.y), hi16(x1.y), lo16(x1.z), hi16(x1.z), lo16(x1.w), hi16(x1.w)};
    v4u xn = ln_row(x);
    sts1(xn, c.rs, l16, OFF_XNB + l * 2 * MB + row * 1024);
    sts1(x1, c.rs, l16, OFF_X1 + l * 2 * MB + row * 1024);
    SCHED();
    ld_u(sn, vn);
    SCHED();
    WAITV(32);
    BAR();
    if (c.w == 0 && c.lane < 8) addflag(c.ws, fl + FL_B + c.lane * 256 + s * 4, 1u);
    TLOG(0x400 + s);
    s = sn;
  }
  DRAIN();
  BAR();
}

template <bool fin>
__device__ __forceinline__ void role_s0(const Ctx& c, int lp, int j0, int stride, rsrc_t ro, const u16* Bo) {
  const unsigned fl = OFF_FLG + lp * FL_SIZE;
  const unsigned l16 = (unsigned)c.lane * 16;
  LAS unsigned char* OF = c.L;
  LAS unsigned char* RS = c.L + 16384;
  LAS unsigned char* WL = c.L + 32768 + c.w * 32768;
  v4u a[96];
  {
    const v4u srd = mksrd(Bo, DM * DM * 2);
    const unsigned v0 = (unsigned)(c.g * 1024 + ((c.fr ^ (2 * c.g)) * 16)), v1 = v0 ^ 128u, lb = 32768u + (unsigned)c.w * 32768;
    const int q = c.fr >> 2, p = c.fr & 3;
    const unsigned ra = lb + (unsigned)((4 * c.g + q) * 256 + (p & 1) * 8 + (p >> 1) * 16 + (q + 4 * (c.g & 1)) * 32);
    auto stage = [&](int n) {
      const int ks = n < 12 ? (n / 3) * 4 + n % 3 : (n - 12) * 4 + 3;
#pragma unroll
      for (int j = 0; j < 8; ++j) dma1(srd, (j & 1) ? v1 : v0, (unsigned)((ks * 32 + 4 * j) * 1024 + c.w * 256), lb + (n & 3) * 8192 + j * 1024);
    };
#pragma unroll
    for (int n = 0; n < 4; ++n) stage(n);
    auto one = [&](auto N) {
      constexpr int n = decltype(N)::value;
      WAITV(n < 13 ? 24 : (15 - n) * 8);
      v4u t[8];
#pragma unroll
      for (int ct = 0; ct < 8; ++ct) t[ct] = trd2(c, (ra ^ (ct * 32)) + (n & 3) * 8192, (ra ^ (ct * 32)) + (n & 3) * 8192 + 4096);
      LGKM();
      if (n < 12) {
        stage(n + 4);
#pragma unroll
        for (int ct = 0; ct < 8; ++ct) { a[(n % 12) * 8 + ct] = t[ct]; if (n < 7) asm volatile("" : "+a"(a[(n % 12) * 8 + ct])); else asm volatile("" : "+v"(a[(n % 12) * 8 + ct])); }
      } else {
#pragma unroll
        for (int ct = 0; ct < 8; ++ct) *(LAS v4u*)(WL + ((n & 3) * 8 + ct) * 1024 + l16) = t[ct];
      }
    };
    static_for<16>(one);
  }
  TLOG2(0x820);
  unsigned cw;
  {
    u64 pc; unsigned len;
    asm volatile("s_getpc_b64 %0\nL_cpg%=:\n s_mov_b32 %1, L_ce1-L_cpg%=\n s_max_i32 %1, %1, 0" : "=s"(pc), "=s"(len) : : "scc");
    cw = __builtin_amdgcn_raw_buffer_load_b32(__builtin_amdgcn_make_buffer_rsrc((void*)pc, 0, len, 0x00020000), (unsigned)c.lane * CPF_ST, (unsigned)c.w * (64 * CPF_ST), 0);
  }
  v4u kvz[8], xr[4];
  v2u pq[8];
  auto ld_p = [&](int j, unsigned vo) {
    const unsigned po = OFF_PQ + (unsigned)((lp * 64 + (j >> 1)) * 32 + 8 * c.w) * 1024 + (j & 1) * 8;
#pragma unroll
    for (int i = 0; i < 8; ++i) pq[i] = __builtin_amdgcn_raw_buffer_load_b64(c.rs, vo + (i & 3) * 1024, po + (i & 4) * 1024, SC1);
  };
  auto ld_x = [&](int j, unsigned vo) {
#pragma unroll
    for (int i = 0; i < 4; ++i) xr[i] = lds1(c.rs, vo + i * 1024, OFF_X1 + lp * 2 * MB + (unsigned)(j * 16 + 4 * c.w) * 1024);
  };
  auto ld_kv = [&](unsigned vo) {
#pragma unroll
    for (int i = 0; i < 8; ++i) kvz[i] = lds1(c.rs, vo + (i & 3) * 1024, OFF_KVF + (unsigned)((fin ? 1 + (blockIdx.x >> 3) % NREP : 0) * 32 + 8 * c.w + (i & 4)) * 1024);
  };
  const unsigned fo = fl + ((c.lane & 1) ? FL_PQ : FL_B) + (blockIdx.x & 7) * 256;
  const bool late = fin && (unsigned)(j0 >> 1) >= FAST;
  if (c.w == 0) {
    unsigned sp = 0;
    while (!__all(ldflag(c.ws, (late ? fl + FL_B + (blockIdx.x & 7) * 256 : fo) + (unsigned)(j0 >> 1) * 4) >= 8u)) if (spin_fail<SL_ST>(sp, c.ws)) break;
  }
  BAR();
  asm volatile("" ::"v"(cw));
  TLOG2(0x821);
  ld_p(j0, l16);
  ld_x(j0, l16);
  if (c.w == 0) {
    unsigned sp = 0;
    while (__builtin_amdgcn_readfirstlane(ldflag(c.ws, fl + FL_B + (blockIdx.x & 7) * 256 + GATE_E * 4)) < 8u) if (spin_fail<SL_P>(sp, c.ws)) break;
    __builtin_amdgcn_s_sleep(SL_E0);
  }
  BAR();
  TLOG2(0x822);
  {
    unsigned sp = 0;
    for (;;) {
      ld_kv(l16);
      unsigned m = 0;
      if (late) {
        ld_p(j0, l16);
#pragma unroll
        for (int i = 0; i < 8; ++i) m = max(m, max(pq[i].x, pq[i].y));
      }
#pragma unroll
      for (int i = 0; i < 8; ++i) m = umax4(m, kvz[i]);
      if (!hole(m) || spin_fail<SL_E>(sp, c.ws)) break;
    }
  }
  for (int j = j0; j < 128; j += stride) {
    TLOG(0x600 + j);
    unsigned nf = 8u;
    if (!fin) nf = ldflag(c.ws, fo + (unsigned)(((j + stride) >> 1) & 63) * 4);
    SCHED();
#pragma unroll
    for (int hf = 0; hf < 2; ++hf) {
      f32x4 num[4], den[4];
#pragma unroll
      for (int i = 0; i < 4; ++i) {
        const v4u kz = kvz[4 * hf + i];
        const f32x4 z4 = {};
        den[i] = mfma16k((v2u){kz.z, kz.w}, pq[4 * hf + i], z4);
        num[i] = mfma16k((v2u){kz.x, kz.y}, pq[4 * hf + i], z4);
      }
      v2u o[4];
#pragma unroll
      for (int i = 0; i < 4; ++i) {
        const float r = __builtin_amdgcn_rcpf(rbf(rbf(den[i][0]) + 1e-6f));
        const v2u n = pack4(num[i]);
        o[i] = (v2u){pack2(lo16(n.x) * r, hi16(n.x) * r), pack2(lo16(n.y) * r, hi16(n.y) * r)};
      }
#pragma unroll
      for (int kk = 0; kk < 2; ++kk) *(LAS v4u*)(OF + (4 * c.w + 2 * hf + kk) * 1024 + l16) = (v4u){o[2 * kk].x, o[2 * kk].y, o[2 * kk + 1].x, o[2 * kk + 1].y};
    }
    SCHED();
    TLOG2(0x810);
    v4u st[8], b[3];
#pragma unroll
    for (int ct = 0; ct < 8; ++ct) st[ct] = *(const LAS v4u*)(WL + ct * 1024 + l16);
    BAR();
    f32x4 acc[8] = {};
    b[0] = *(const LAS v4u*)(OF + l16);
    b[1] = *(const LAS v4u*)(OF + 1024 + l16);
    FENCE();
    auto step = [&](auto KS) {
      constexpr int ks = decltype(KS)::value;
      const v4u bc = b[ks % 3];
#pragma unroll
      for (int ct = 0; ct < 8; ++ct) {
        acc[ct] = mfma16((ks & 3) == 3 ? st[ct] : a[((ks >> 2) * 3 + (ks & 3) % 3) * 8 + ct], bc, acc[ct]);
        if (ct == 0 && ks + 2 < 16) b[(ks + 2) % 3] = *(const LAS v4u*)(OF + (ks + 2) * 1024 + l16);
        if ((ks & 3) == 0 && ks > 0) st[ct] = *(const LAS v4u*)(WL + ((ks >> 2) * 8 + ct) * 1024 + l16);
        FENCE();
      }
    };
    static_for<16>(step);
    TLOG2(0x811);
    int ln = c.lane;
    asm volatile("" : "+v"(ln));
    const int fr = ln & 15, g = ln >> 4;
#pragma unroll
    for (int ct = 0; ct < 8; ++ct) *(LAS v2u*)(RS + fr * 1024 + (((16 * c.w + 2 * ct + (g >> 1)) ^ fr) * 16) + (g & 1) * 8) = pack4(acc[ct]);
#pragma unroll
    for (int i = 0; i < 4; ++i) LANDED(xr[i]);
    SCHED();
    BAR();
    v4u x2[4];
#pragma unroll
    for (int i = 0; i < 4; ++i) x2[i] = *(const LAS v4u*)(RS + (4 * c.w + i) * 1024 + ((ln ^ (4 * c.w + i)) * 16));
    SCHED();
#pragma unroll
    for (int i = 0; i < 4; ++i) x2[i] = addbf(xr[i], x2[i]);
    const unsigned ro_ = (unsigned)(j * 16 + 4 * c.w) * 1024;
    if (fin) {
#pragma unroll
      for (int i = 0; i < 4; ++i) __builtin_amdgcn_raw_buffer_store_b128(x2[i], ro, l16 + i * 1024, ro_, 0);
      TLOG(0x200 + j);
      break;
    }
    v4u xn[4];
    if (j < 8 && c.w == 0) __builtin_amdgcn_raw_buffer_store_b16((short)1, c.rs, ln < 32 ? (unsigned)ln * 256 : OOB, OFF_FLG + FL_SIZE + FL_X + (unsigned)j * 2, SC1);
    SCHED();
    ln_rows<4>(x2, xn);
#pragma unroll
    for (int i = 0; i < 4; ++i) sts1(xn[i], c.rs, l16 + i * 1024, OFF_XNA + 2 * MB + ro_);
#pragma unroll
    for (int i = 0; i < 4; ++i) sts1(x2[i], c.rs, l16 + i * 1024, OFF_X2 + ro_);
    SCHED();
    const unsigned vn = j + stride < 128 ? l16 : OOB;
    {
      unsigned sp = 0;
      while (!__all(nf >= 8u)) { nf = ldflag(c.ws, fo + (unsigned)(((j + stride) >> 1) & 63) * 4); if (spin_fail<SL_P>(sp, c.ws)) break; }
    }
    ld_kv(vn);
    ld_p(j + stride, vn);
    ld_x(j + stride, vn);
    SCHED();
    TLOG2(0x812);
    WAITV(20);
    BAR();
    if (c.w == 0) __builtin_amdgcn_raw_buffer_store_b16((short)3, c.rs, ln < 32 ? (unsigned)ln * 256 : OOB, OFF_FLG + FL_SIZE + FL_X + (unsigned)j * 2, SC1);
    TLOG(0x200 + j);
  }
}

constexpr int PH_S0 = 1, PH_S1 = 2, PH_REC = 4, PH_S2 = 8, PH_S3B = 16;
__device__ __forceinline__ void worker(unsigned char* ws, const u16* xin, u16* out, const u16* Wq, const u16* Wk, const u16* Wv, const u16* Wo, const u16* Bq, const u16* Bo, int l0, int l1, int ph, int w
    , const unsigned (&cv)[3]
    ) {
  extern __shared__ __attribute__((aligned(16))) unsigned char smem[];
  Ctx c;
  c.ws = ws;
  c.rs = __builtin_amdgcn_make_buffer_rsrc(ws, 0, WS_END, 0x00020000);
  const u64 wa = (u64)ws;
  c.srd = (v4u){(unsigned)wa, (unsigned)(wa >> 32), WS_END, 0x00020000u};
  c.L = (LAS unsigned char*)smem;
  c.w = w;
  const int wi = blockIdx.x - 128, cls = wi & 7, idx = wi >> 3;
  const rsrc_t rin = __builtin_amdgcn_make_buffer_rsrc((void*)xin, 0, S_TOK * DM * 2, 0x00020000);
  const rsrc_t rout = __builtin_amdgcn_make_buffer_rsrc(out, 0, S_TOK * DM * 2, 0x00020000);
  c.tln = 0;
  auto fresh = [&]() { __builtin_amdgcn_s_waitcnt(0x0F70); int ln; asm volatile("v_mbcnt_lo_u32_b32 %0, -1, 0\n v_mbcnt_hi_u32_b32 %0, -1, %0" : "=v"(ln)); c.lane = ln; c.fr = ln & 15; c.g = ln >> 4; };
  if (wi < 0) {
    if (l1 > 2 && (ph & PH_S0)) {
      int ln; asm volatile("v_mbcnt_lo_u32_b32 %0, -1, 0\n v_mbcnt_hi_u32_b32 %0, -1, %0" : "=v"(ln)); c.lane = ln; c.fr = ln & 15; c.g = ln >> 4;
      TLOG(0x100);
      role_s0<true>(c, 1, wi + 128, 128, rout, Bo);
      asm volatile("L_ce1:" ::: "memory");
    }
    return;
  }
  TLOG(1);
  if (l0 == 0 && wi >= 0 && wi < 8) {
    { int ln; asm volatile("v_mbcnt_lo_u32_b32 %0, -1, 0\n v_mbcnt_hi_u32_b32 %0, -1, %0" : "=v"(ln)); c.lane = ln; c.fr = ln & 15; c.g = ln >> 4; }
    const unsigned l16 = (unsigned)c.lane * 16, tv = c.lane < 4 ? (unsigned)c.lane * 256 : OOB;
    unsigned z = 0;
    asm volatile("" : "+v"(z));
    for (unsigned u = wi * 4 + c.w; u < (4096 + 2 * FL_SIZE) / 1024; u += 32) sts1((v4u){z, z, z, z}, c.rs, l16, OFF_CTL + u * 1024);
    DRAIN();
    asm volatile("" ::"v"(cv[0]), "v"(cv[1]), "v"(cv[2]));
    DRAIN();
    BAR();
    TLOG(7);
    if (c.w == 3) {
      for (unsigned sp = 0; sp < 400000u; ++sp) {
        asm volatile("" ::: "memory");
        const unsigned n = ldflag(ws, OFF_CTL + wi * 4096 + 48);
#pragma unroll
        for (int k = 0; k < 4; ++k) { __builtin_amdgcn_raw_buffer_store_b32(sp * 4 + k, c.rs, tv, OFF_HS + wi * 1024, SC1); __builtin_amdgcn_s_sleep(10); asm volatile("" ::: "memory"); }
        if (__builtin_amdgcn_readfirstlane(n) >= 256u) break;
      }
    }
  }
  for (int l = l0; l < l1; ++l) {
    if (l == 2) break;
    if (l > 0 && (ph & PH_S0) && wi < 32) { fresh(); TLOG(0x100); role_s0<false>(c, 0, wi, 32, rout, Bo); }
    TLOG(2);
    fresh();
    if (ph & PH_S1) {
      if (idx < 8) role_s1<false>(c, l, idx >> 2, cls + 8 * (idx & 3), 0, idx < 4 ? Wq : Wk, Wo, xin);
      else role_s1<true>(c, l, 2, cls + 8 * (idx & 3), (idx >> 2) & 1, Wv, Wo, xin);
    }
    TLOG(3);
    fresh();
    if (idx >= 12) { if (ph & PH_S3B) role_s3b(c, l, cls, idx - 12, Bq); TLOG(4); }
    else if (ph & PH_S2) role_s2(c, l, cls, idx, l == 0 ? rin : c.rs, l == 0 ? 0u : OFF_X2);
    if (l == 1 && idx < WB_N) {
      fresh();
      const v4u srd = mksrd(Bo, DM * DM * 2);
#pragma unroll 4
      for (int j = 0; j < 128 / WB_N; ++j) dma1(srd, (unsigned)c.lane * 16, (unsigned)((j * WB_N + idx) * 4 + c.w) * 1024, (unsigned)(c.w * 16 + (j & 15)) * 1024);
      DRAIN();
    }
  }
}

constexpr int MEGA_LDS = 163840;
__global__ __launch_bounds__(256, 1) void k_mega(unsigned char* ws, const u16* xin, const u16* mem0, u16* out, const u16* Wq, const u16* Wk, const u16* Wv,
                                                    const u16* Wo, const u16* Bq, const u16* Bo, int l0, int l1, int ph) {
  const int bx = blockIdx.x;
  const int w = __builtin_amdgcn_readfirstlane(threadIdx.x >> 6);
  unsigned cv[3] = {0u, 0u, 0u};
  {
    u64 pc; unsigned len;
    asm volatile("s_getpc_b64 %0\nL_cp0:\n s_mov_b32 %1, L_ce1-L_cp0" : "=s"(pc), "=s"(len));
    const rsrc_t rc = __builtin_amdgcn_make_buffer_rsrc((void*)pc, 0, (unsigned)(bx - 128) < 8u ? len : 0u, 0x00020000);
#pragma unroll
    for (int k = 0; k < 3; ++k) cv[k] = __builtin_amdgcn_raw_buffer_load_b32(rc, threadIdx.x * 128u, k * 32768, 0);
    __builtin_amdgcn_sched_barrier(0);
  }
  if (bx < 128 && (ph & PH_REC) && l0 < 2) {
  const unsigned h = (bx & 7) + 8 * ((bx >> 3) & 3), cq = bx >> 5;
  const int r1 = l1 < 2 ? l1 : 2;
  const unsigned lead = __builtin_amdgcn_readfirstlane((bx < 8 && w == 3) ? 1u : 0u);
  const unsigned oAt = 32768 + (2 * cq + (w >> 1)) * 4096 + (w & 1) * 1024;
  const unsigned oKx = OFF_KF + h * 8192 + w * 2048, oU = OFF_UF + (h * 32 + 8 * cq + 2 * w) * 1024;
  const unsigned oY = OFF_YF + (h * 16 + 4 * cq + w) * 512, oKm = OFF_KM + h * 2048 + (cq * 4 + w) * 128;
  const unsigned oFl = OFF_FLG + FL_QKV + h * 64, oPb = OFF_FLG + FL_R + (h & 7) * 128, oWarm = bx * 16384;
  asm volatile("s_waitcnt vmcnt(0)\nL_cbeg:\n" REC_ASM_BODY
               :
               : [w] "s"(w), [cq] "s"(cq), [ws] "s"(ws), [pMem0] "s"(mem0), [h] "s"(h), [pWq] "s"(Wq), [pWk] "s"(Wk), [pWv] "s"(Wv), [pWo] "s"(Wo), [l0] "s"(l0), [l1] "s"(r1),
                 [lead] "s"(lead), [oAt] "s"(oAt), [oKx] "s"(oKx), [oU] "s"(oU), [oY] "s"(oY), [oKm] "s"(oKm),
                 [oFl] "s"(oFl), [oPb] "s"(oPb), [oWarm] "s"(oWarm), [wsz] "n"(WS_END)
               : REC_ASM_CLOBBERS);
  cv[0] = cv[1] = cv[2] = 0u;
  }
  worker(ws, xin, out, Wq, Wk, Wv, Wo, Bq, Bo, bx < 128 ? 2 : l0, l1, ph, w, cv);
}

extern "C" void kernel_launch(void* const* d_in, const int* in_sizes, int n_in, void* d_out, int out_size, void* d_ws, size_t ws_size,
                              hipStream_t stream) {
  const u16* A_x = (const u16*)d_in[0];
  const u16* A_mem0 = (const u16*)d_in[1];
  const u16* A_Wq = (const u16*)d_in[2];
  const u16* A_Wk = (const u16*)d_in[3];
  const u16* A_Wv = (const u16*)d_in[4];
  const u16* A_Wo = (const u16*)d_in[5];
  const u16* B_wqkv = (const u16*)d_in[8];
  const u16* B_wo = (const u16*)d_in[9];
  unsigned char* ws = (unsigned char*)d_ws;
  u16* out = (u16*)d_out;
  hipFuncSetAttribute((const void*)k_mega, hipFuncAttributeMaxDynamicSharedMemorySize, MEGA_LDS);
  k_mega<<<256, 256, MEGA_LDS, stream>>>(ws, A_x, A_mem0, out, A_Wq, A_Wk, A_Wv, A_Wo, B_wqkv, B_wo, 0, 3, PH_S0 | PH_S1 | PH_REC | PH_S2 | PH_S3B);
}
```
